```python
import torch

def _softmax(x, axis=-1):
    return torch.softmax(x, dim=axis)
def _gelu(x):
    return torch.nn.functional.gelu(x, approximate="tanh")
def _silu(x):
    return torch.nn.functional.silu(x)
def _sigmoid(x):
    return torch.sigmoid(x)
def _layernorm(x, axis=-1, eps=1e-5):
    m = x.mean(dim=axis, keepdim=True)
    v = x.var(dim=axis, keepdim=True, unbiased=False)
    return (x - m) / torch.sqrt(v + eps)
def _scatter_add(tgt, idx, src, axis=0):
    return tgt.clone().index_add_(axis, idx, src)
def _segment_sum(src, seg, n):
    out = torch.zeros((n,) + src.shape[1:], dtype=src.dtype)
    return out.index_add_(0, seg, src)
def _pad(x, pw):
    flat = tuple(v for pair in reversed(pw) for v in pair)
    return torch.nn.functional.pad(x, flat)

kernel_name = 'gated_mix__mha_block_cached__mixer_channel_mlp__decode'

def setup_inputs():

    _w = (lambda a: (lambda t: t.to(torch.bfloat16) if t.is_floating_point() else t)(torch.as_tensor(a)))
    def _A_setup():
        B, Sq, D = 8, 1, 576
        H, Sk, Dh = 32, 4096, 128
        return {
            "x": _w((lambda *s: torch.randn(*s, dtype=torch.bfloat16) * 0.05)(B, Sq, D)),
            "k_cache": _w((lambda *s: torch.randn(*s, dtype=torch.bfloat16) * 6.4)(B, H, Sk, Dh)),
            "v_cache": _w((lambda *s: torch.randn(*s, dtype=torch.bfloat16) * 0.4)(B, H, Sk, Dh)),
            "wq": _w((lambda *s: torch.randn(*s, dtype=torch.bfloat16) * 6.4)(D, H * Dh) * (D ** -0.5)),
            "wo": _w((lambda *s: torch.randn(*s, dtype=torch.bfloat16) * 0.4)(H * Dh, D) * ((H * Dh) ** -0.5)),
        }
    _A_in = _A_setup()

    def _B_setup():
        B, S, D, F = 8, 1, 576, 4608
        return {
            "x": _w((lambda *s: torch.randn(*s, dtype=torch.bfloat16) * 0.05)(B, S, D)),
            "w1": _w((lambda *s: torch.randn(*s, dtype=torch.bfloat16) * 0.8)(D, F) * (D ** -0.5)),
            "b1": _w((lambda *s: torch.randn(*s, dtype=torch.bfloat16) * 0.8)(F)),
            "w2": _w((lambda *s: torch.randn(*s, dtype=torch.bfloat16) * 0.05)(F, D) * (F ** -0.5)),
            "b2": _w((lambda *s: torch.randn(*s, dtype=torch.bfloat16) * 0.05)(D)),
        }
    _B_in = _B_setup()

    return {
        "A_x": _A_in["x"],
        "A_k_cache": _A_in["k_cache"],
        "A_v_cache": _A_in["v_cache"],
        "A_wq": _A_in["wq"],
        "A_wo": _A_in["wo"],
        "B_x": _B_in["x"],
        "B_w1": _B_in["w1"],
        "B_b1": _B_in["b1"],
        "B_w2": _B_in["w2"],
        "B_b2": _B_in["b2"],
        "gate_w": _w((lambda *s: torch.randn(*s, dtype=torch.bfloat16) * 0.05)(576) * 16.0),
    }

def reference(A_x, A_k_cache, A_v_cache, A_wq, A_wo, B_x, B_w1, B_b1, B_w2, B_b2, gate_w):
    def _A_ref(x, k_cache, v_cache, wq, wo):
        B, Sq, D = x.shape
        H, Dh = 32, 128
        q = (lambda x, *d: x.permute(*d))((x @ wq).reshape(B, Sq, H, Dh), 0, 2, 1, 3)
        scores = torch.einsum("bhqd,bhkd->bhqk", q, k_cache) * (Dh ** -0.5)
        p = _softmax(scores, axis=-1)
        o = torch.einsum("bhqk,bhkd->bhqd", p, v_cache)
        return (lambda x, *d: x.permute(*d))(o, 0, 2, 1, 3).reshape(B, Sq, H * Dh) @ wo

    def _B_ref(x, w1, b1, w2, b2):
        y = _layernorm(x, axis=-1)
        h = _gelu(torch.einsum("bsd,df->bsf", y, w1) + b1)
        return torch.einsum("bsf,fd->bsd", h, w2) + b2

    x = A_x
    g = _sigmoid((x * gate_w).sum(-1, keepdims=True))
    a = _A_ref(x, A_k_cache, A_v_cache, A_wq, A_wo)
    b = _B_ref(x, B_w1, B_b1, B_w2, B_b2)
    return g * a + (1 - g) * b
```

```cpp
#include <hip/hip_runtime.h>
#include <hip/hip_bf16.h>
#include <stdint.h>

namespace gm {

typedef unsigned v4u __attribute__((ext_vector_type(4)));
typedef float v4f __attribute__((ext_vector_type(4)));
typedef __bf16 bf16x2 __attribute__((ext_vector_type(2)));
typedef unsigned short u16;
typedef __amdgpu_buffer_rsrc_t rsrc_t;

constexpr int NB = 8, NH = 32, SK = 4096, DH = 128, DM = 576, DF = 4608;
constexpr int NS = 8;
constexpr int NA = 8;
constexpr int NT = (NS + NA) * 64;
constexpr int W = 16;
constexpr int FCH = DF / NH;
constexpr int AUX_NT = 2;

#define PIN(v) asm volatile("" : "+v"(v))
#define PIN8(a) asm volatile("" : "+v"(a[0]), "+v"(a[1]), "+v"(a[2]), "+v"(a[3]), "+v"(a[4]), "+v"(a[5]), "+v"(a[6]), "+v"(a[7]))
#define SCHED_FENCE() __builtin_amdgcn_sched_barrier(0)

__device__ __forceinline__ rsrc_t mk_rsrc(const void* p, unsigned bytes) {
  return __builtin_amdgcn_make_buffer_rsrc(const_cast<void*>(p), 0, bytes, 0x00020000);
}
template <int AUX> __device__ __forceinline__ v4u ld16(rsrc_t r, int voff, int soff) {
  return __builtin_amdgcn_raw_buffer_load_b128(r, voff, soff, AUX);
}
__device__ __forceinline__ unsigned ld2(rsrc_t r, int voff, int soff) {
  return __builtin_amdgcn_raw_buffer_load_b16(r, voff, soff, 0);
}
__device__ __forceinline__ float lo16(unsigned u) { return __builtin_bit_cast(float, u << 16); }
__device__ __forceinline__ float hi16(unsigned u) { return __builtin_bit_cast(float, u & 0xffff0000u); }
__device__ __forceinline__ float dot2(unsigned a, unsigned b, float acc) {
  return __builtin_amdgcn_fdot2_f32_bf16(__builtin_bit_cast(bf16x2, a), __builtin_bit_cast(bf16x2, b), acc, false);
}
__device__ __forceinline__ unsigned pack_bf16(float lo, float hi) {
  bf16x2 r;
  r[0] = (__bf16)lo;
  r[1] = (__bf16)hi;
  return __builtin_bit_cast(unsigned, r);
}
template <int CTRL> __device__ __forceinline__ float dpp(float x) {
  return __builtin_bit_cast(float, __builtin_amdgcn_mov_dpp(__builtin_bit_cast(int, x), CTRL, 0xf, 0xf, true));
}
constexpr int XOR1 = 0xB1, XOR2 = 0x4E, XOR7 = 0x141, XOR8 = 0x128;
__device__ __forceinline__ float sum_x16_x32(float x) {
  auto s = __builtin_amdgcn_permlane16_swap(__float_as_uint(x), __float_as_uint(x), false, false);
  x = __uint_as_float(s[0]) + __uint_as_float(s[1]);
  auto t = __builtin_amdgcn_permlane32_swap(__float_as_uint(x), __float_as_uint(x), false, false);
  return __uint_as_float(t[0]) + __uint_as_float(t[1]);
}
__device__ __forceinline__ float max_x16_x32(float x) {
  auto s = __builtin_amdgcn_permlane16_swap(__float_as_uint(x), __float_as_uint(x), false, false);
  x = fmaxf(__uint_as_float(s[0]), __uint_as_float(s[1]));
  auto t = __builtin_amdgcn_permlane32_swap(__float_as_uint(x), __float_as_uint(x), false, false);
  return fmaxf(__uint_as_float(t[0]), __uint_as_float(t[1]));
}
__device__ __forceinline__ float row_sum16(float x) {
  x += dpp<XOR8>(x); x += dpp<XOR7>(x); x += dpp<XOR2>(x); x += dpp<XOR1>(x);
  return x;
}
__device__ __forceinline__ float row_max16(float x) {
  x = fmaxf(x, dpp<XOR8>(x)); x = fmaxf(x, dpp<XOR7>(x)); x = fmaxf(x, dpp<XOR2>(x)); x = fmaxf(x, dpp<XOR1>(x));
  return x;
}
__device__ __forceinline__ float wave_sum(float x) { return sum_x16_x32(row_sum16(x)); }
__device__ __forceinline__ float wave_max(float x) { return max_x16_x32(row_max16(x)); }
__device__ __forceinline__ float rdlane(float x, int l) {
  return __builtin_bit_cast(float, __builtin_amdgcn_readlane(__builtin_bit_cast(int, x), l));
}
__device__ __forceinline__ void fma8(float (&acc)[8], float s, v4u w) {
#pragma unroll
  for (int e = 0; e < 4; ++e) {
    const unsigned u = w[e];
    acc[2 * e] = fmaf(s, lo16(u), acc[2 * e]);
    acc[2 * e + 1] = fmaf(s, hi16(u), acc[2 * e + 1]);
  }
}
#define FMA8_HERE(acc, s, w) do { PIN(w); fma8(acc, s, w); PIN8(acc); SCHED_FENCE(); } while (0)

constexpr int L_Q = 0;
constexpr int L_PART = L_Q + 16 * 128;
constexpr int L_ML = L_PART + NS * 128;
constexpr int L_Y = L_ML + 32;
constexpr int L_HP = L_Y + NA * 72;
constexpr int L_OUT = L_HP + NA * 3 * FCH;
constexpr int L_CNT = L_OUT + NA * DM;
constexpr int L_END = L_CNT + 4;

__device__ __forceinline__ void q_loads(rsrc_t rx, rsrc_t rwq, int h, int wave, int lane, unsigned (&xr)[9], v4u (&wv)[9]) {
  const int g = lane >> 4, c = lane & 15;
#pragma unroll
  for (int i = 0; i < 9; ++i) xr[i] = ld2(rx, g * 2, wave * 72 + i * 8);
#pragma unroll
  for (int i = 0; i < 9; ++i) wv[i] = ld16<0>(rwq, g * (NH * DH * 2) + c * 16, (36 * wave + 4 * i) * (NH * DH * 2) + h * (DH * 2));
  SCHED_FENCE();
}
__device__ __forceinline__ void q_partial(unsigned (&xr)[9], v4u (&wv)[9], float* lds, int wave, int lane) {
  float qa[8];
#pragma unroll
  for (int e = 0; e < 8; ++e) qa[e] = 0.f;
#pragma unroll
  for (int i = 0; i < 9; ++i) FMA8_HERE(qa, lo16(xr[i]), wv[i]);
#pragma unroll
  for (int e = 0; e < 8; ++e) qa[e] = sum_x16_x32(qa[e]);
  if (lane < 16) {
    v4f* d = (v4f*)(lds + L_Q + wave * 128 + lane * 8);
    d[0] = v4f{qa[0], qa[1], qa[2], qa[3]};
    d[1] = v4f{qa[4], qa[5], qa[6], qa[7]};
  }
}

__global__ void __launch_bounds__(NT, 4)
fused_main(const u16* __restrict__ x, const u16* __restrict__ kc, const u16* __restrict__ vc,
           const u16* __restrict__ wq, const u16* __restrict__ wo, const u16* __restrict__ w1,
           const u16* __restrict__ b1, const u16* __restrict__ w2, const u16* __restrict__ b2,
           const u16* __restrict__ gw, float* __restrict__ part) {
  __shared__ __attribute__((aligned(16))) float lds[L_END];
  const int bid = blockIdx.x, b = bid >> 5, h = bid & 31;
  const int tid = threadIdx.x;
  const int wave = __builtin_amdgcn_readfirstlane(tid >> 6);
  asm volatile("" :: "s"(x), "s"(kc), "s"(vc), "s"(wq), "s"(w1));
  const rsrc_t rx = mk_rsrc(x + b * DM, DM * 2);
  const rsrc_t rwq = mk_rsrc(wq, DM * NH * DH * 2);
  if (tid == 0) *(unsigned*)(lds + L_CNT) = 0u;

  if (wave < NS) {
    int lane = tid & 63;
    asm volatile("; streaming" : "+v"(lane) :: "memory");
    const int g = lane >> 4, c = lane & 15;
    unsigned xq[9];
    v4u wv[9];
    q_loads(rx, rwq, h, wave, lane, xq, wv);
    constexpr int WBYTES = SK / NS * DH * 2;
    const size_t base = ((size_t)bid * NS + wave) * (WBYTES / 2);
    const rsrc_t rk = mk_rsrc(kc + base, WBYTES);
    const rsrc_t rv = mk_rsrc(vc + base, WBYTES);
    const int vo = lane * 16;
    v4u buf[W];
#pragma unroll
    for (int j = 0; j < W; ++j) buf[j] = ld16<AUX_NT>(rk, vo, j * 1024);
    SCHED_FENCE();
    q_partial(xq, wv, lds, wave, lane);
    __syncthreads();
    unsigned qp[4];
    {
      float qa[8];
#pragma unroll
      for (int e = 0; e < 8; ++e) qa[e] = 0.f;
#pragma unroll
      for (int w = 0; w < 4; ++w) {
        const v4f* s = (const v4f*)(lds + L_Q + (4 * g + w) * 128 + c * 8);
        const v4f a0 = s[0], a1 = s[1];
#pragma unroll
        for (int e = 0; e < 4; ++e) { qa[e] += a0[e]; qa[4 + e] += a1[e]; }
      }
#pragma unroll
      for (int e = 0; e < 8; ++e) qa[e] = sum_x16_x32(qa[e]);
#pragma unroll
      for (int e = 0; e < 4; ++e) qp[e] = pack_bf16(qa[2 * e], qa[2 * e + 1]);
    }
    constexpr int NBLK = SK / NS / 4 / W;
    float s[NBLK];
#pragma unroll
    for (int i = 0; i < NBLK; ++i) s[i] = 0.f;
#pragma unroll 1
    for (int blk = 0; blk < NBLK; ++blk) {
      const bool last = blk + 1 == NBLK;
      const rsrc_t rn = last ? rv : rk;
      const int so = last ? 0 : (blk + 1) * (W * 1024);
      float xs[W];
#pragma unroll
      for (int j = 0; j < W; ++j) {
        const v4u cur = buf[j];
        float d = dot2(cur[0], qp[0], 0.f);
        d = dot2(cur[1], qp[1], d);
        d = dot2(cur[2], qp[2], d);
        d = dot2(cur[3], qp[3], d);
        PIN(d);
        xs[j] = d;
        buf[j] = ld16<AUX_NT>(rn, vo, so + j * 1024);
        SCHED_FENCE();
      }
#define TR_STEP(HALF, CTRL)                                         \
  _Pragma("unroll") for (int u = 0; u < (HALF); ++u) {              \
    const float lo_ = xs[u], hi_ = xs[u + (HALF)];                  \
    const float t1_ = lo_ + dpp<CTRL>(lo_), t2_ = hi_ + dpp<CTRL>(hi_); \
    xs[u] = (c & (HALF)) ? t2_ : t1_;                               \
  }
      TR_STEP(8, XOR8) TR_STEP(4, XOR7) TR_STEP(2, XOR2) TR_STEP(1, XOR1)
#undef TR_STEP
#pragma unroll
      for (int i = 0; i + 1 < NBLK; ++i) s[i] = s[i + 1];
      s[NBLK - 1] = xs[0];
    }
    constexpr float C2 = 0.088388347648318440f * 1.4426950408889634f;
    float m = s[0];
#pragma unroll
    for (int i = 1; i < NBLK; ++i) m = fmaxf(m, s[i]);
    m = wave_max(m);
    float l = 0.f;
#pragma unroll
    for (int i = 0; i < NBLK; ++i) { s[i] = __builtin_amdgcn_exp2f((s[i] - m) * C2); l += s[i]; }
    l = wave_sum(l);
    float acc[8];
#pragma unroll
    for (int e = 0; e < 8; ++e) acc[e] = 0.f;
#define V_STEP(J, REFILL)                                           \
  {                                                                 \
    fma8(acc, dpp<0x150 + (J)>(p), buf[J]);                         \
    PIN8(acc);                                                      \
    if (REFILL) buf[J] = ld16<AUX_NT>(rv, vo, so + (J) * 1024);     \
    SCHED_FENCE();                                                  \
  }
#define V_BLOCK(REFILL)                                             \
  V_STEP(0, REFILL) V_STEP(1, REFILL) V_STEP(2, REFILL) V_STEP(3, REFILL)     \
  V_STEP(4, REFILL) V_STEP(5, REFILL) V_STEP(6, REFILL) V_STEP(7, REFILL)     \
  V_STEP(8, REFILL) V_STEP(9, REFILL) V_STEP(10, REFILL) V_STEP(11, REFILL)   \
  V_STEP(12, REFILL) V_STEP(13, REFILL) V_STEP(14, REFILL) V_STEP(15, REFILL)
#pragma unroll 1
    for (int blk = 0; blk + 1 < NBLK; ++blk) {
      const int so = (blk + 1) * (W * 1024);
      const float p = s[0];
      V_BLOCK(true)
#pragma unroll
      for (int i = 0; i + 1 < NBLK; ++i) s[i] = s[i + 1];
    }
    {
      const int so = 0;
      const float p = s[0];
      V_BLOCK(false)
    }
#undef V_BLOCK
#undef V_STEP
#pragma unroll
    for (int e = 0; e < 8; ++e) acc[e] = sum_x16_x32(acc[e]);
    if (lane < 16) {
      v4f* d = (v4f*)(lds + L_PART + wave * 128 + lane * 8);
      d[0] = v4f{acc[0], acc[1], acc[2], acc[3]};
      d[1] = v4f{acc[4], acc[5], acc[6], acc[7]};
      if (lane == 0) { lds[L_ML + 2 * wave] = m * C2; lds[L_ML + 2 * wave + 1] = l; }
    }
    __syncthreads();
    __syncthreads();
  } else {
    int lane = tid & 63;
    asm volatile("; helper" : "+v"(lane) :: "memory");
    const int g = lane >> 4, c = lane & 15;
    unsigned xq[9];
    v4u wv[9];
    q_loads(rx, rwq, h, wave, lane, xq, wv);
    const int a = wave - NS;
    const int fch = (h & 7) * 4 + (h >> 3);
    const int f0 = fch * FCH;
    const int lq = lane < 54 ? lane : 53;
    const int grp = lq / 18, ch = lq - 18 * grp;
    const rsrc_t rw1 = mk_rsrc(w1, DM * DF * 2);
    const int w1v = grp * (DF * 2) + ch * 16, w1s = 72 * a * (DF * 2) + f0 * 2;
    v4u wb[12];
#pragma unroll
    for (int i = 0; i < 12; ++i) wb[i] = ld16<0>(rw1, w1v, w1s + i * (3 * DF * 2));
    SCHED_FENCE();
    q_partial(xq, wv, lds, wave, lane);
    __syncthreads();
    SCHED_FENCE();
    const rsrc_t rg = mk_rsrc(gw, DM * 2);
    unsigned xr[9], gr[9];
#pragma unroll
    for (int i = 0; i < 9; ++i) { xr[i] = ld2(rx, lane * 2, i * 128); gr[i] = ld2(rg, lane * 2, i * 128); }
    const unsigned xy0 = ld2(rx, lane * 2, 144 * a);
    const unsigned xy1 = ld2(rx, (lane & 7) * 2, 144 * a + 128);
    SCHED_FENCE();
    float s1 = 0.f, gd = 0.f;
#pragma unroll
    for (int i = 0; i < 9; ++i) { const float v = lo16(xr[i]); s1 += v; gd = fmaf(v, lo16(gr[i]), gd); }
    s1 = wave_sum(s1);
    gd = wave_sum(gd);
    const float mean = s1 * (1.0f / DM);
    float s2 = 0.f;
#pragma unroll
    for (int i = 0; i < 9; ++i) { const float v = lo16(xr[i]) - mean; s2 = fmaf(v, v, s2); }
    s2 = wave_sum(s2);
    const float rstd = 1.0f / sqrtf(s2 * (1.0f / DM) + 1e-5f);
    const float gate = 1.0f / (1.0f + __expf(-gd));
    float* ly = lds + L_Y + a * 72;
    ly[lane] = (lo16(xy0) - mean) * rstd;
    if (lane < 8) ly[64 + lane] = (lo16(xy1) - mean) * rstd;
    __builtin_amdgcn_fence(__ATOMIC_RELEASE, "wavefront");
    float ha[8];
#pragma unroll
    for (int e = 0; e < 8; ++e) ha[e] = 0.f;
#pragma unroll
    for (int i = 0; i < 12; ++i) {
      FMA8_HERE(ha, ly[3 * i + grp], wb[i]);
      wb[i] = ld16<0>(rw1, w1v, w1s + (12 + i) * (3 * DF * 2));
      SCHED_FENCE();
    }
#pragma unroll
    for (int i = 0; i < 12; ++i) FMA8_HERE(ha, ly[36 + 3 * i + grp], wb[i]);
    v4u wm[18], wx[3];
    const int xoff = (lane >> 3) * (DM * 2) + 1024 + (lane & 7) * 16;
    {
      const rsrc_t r = mk_rsrc(w2 + (size_t)(f0 + 18 * a) * DM, 18 * DM * 2);
#pragma unroll
      for (int i = 0; i < 18; ++i) wm[i] = ld16<0>(r, lane * 16, i * (DM * 2));
#pragma unroll
      for (int j = 0; j < 3; ++j) wx[j] = ld16<0>(r, xoff + j * (8 * DM * 2), 0);
    }
    const float bias1 = lo16(ld2(mk_rsrc(b1, DF * 2), ch * 2, (f0 + 18 * a) * 2));
    SCHED_FENCE();
    if (lane < 54) {
      v4f* d = (v4f*)(lds + L_HP + (a * 3 + grp) * FCH + ch * 8);
      d[0] = v4f{ha[0], ha[1], ha[2], ha[3]};
      d[1] = v4f{ha[4], ha[5], ha[6], ha[7]};
    }
    {
      unsigned* cnt = (unsigned*)(lds + L_CNT);
      __builtin_amdgcn_fence(__ATOMIC_RELEASE, "workgroup");
      if (lane == 0) __hip_atomic_fetch_add(cnt, 1u, __ATOMIC_RELAXED, __HIP_MEMORY_SCOPE_WORKGROUP);
      while (__builtin_amdgcn_readfirstlane(__hip_atomic_load(cnt, __ATOMIC_RELAXED, __HIP_MEMORY_SCOPE_WORKGROUP)) < (unsigned)NA)
        __builtin_amdgcn_s_sleep(2);
      __builtin_amdgcn_fence(__ATOMIC_ACQUIRE, "workgroup");
    }
    float hv = 0.f;
#pragma unroll
    for (int w = 0; w < NA; ++w) hv += lds[L_HP + (w * 3 + grp) * FCH + 18 * a + ch];
    hv = hv + __shfl(hv, (lane + 18) & 63) + __shfl(hv, (lane + 36) & 63);
    hv += bias1;
    {
      const float u = 0.7978845608028654f * (hv + 0.044715f * hv * hv * hv);
      hv = hv / (1.0f + __expf(-2.0f * u));
    }
    hv *= (1.0f - gate);
    float am[8], ax[8];
#pragma unroll
    for (int e = 0; e < 8; ++e) { am[e] = 0.f; ax[e] = 0.f; }
#pragma unroll
    for (int i = 0; i < 18; ++i) FMA8_HERE(am, rdlane(hv, i), wm[i]);
#pragma unroll
    for (int j = 0; j < 3; ++j) {
      const int r = (lane >> 3) + 8 * j;
      float hr = __shfl(hv, r < 18 ? r : 0);
      hr = r < 18 ? hr : 0.f;
      FMA8_HERE(ax, hr, wx[j]);
    }
    if (h == 0 && a == 0) {
      const rsrc_t r = mk_rsrc(b2, DM * 2);
      v4u bm = ld16<0>(r, lane * 16, 0);
      v4u bx = ld16<0>(r, xoff, 0);
      FMA8_HERE(am, 1.0f - gate, bm);
      FMA8_HERE(ax, 1.0f - gate, bx);
    }
    v4u om[16], ox[2];
    {
      const rsrc_t r = mk_rsrc(wo + (size_t)(h * DH + 16 * a) * DM, 16 * DM * 2);
#pragma unroll
      for (int i = 0; i < 16; ++i) om[i] = ld16<0>(r, lane * 16, i * (DM * 2));
#pragma unroll
      for (int j = 0; j < 2; ++j) ox[j] = ld16<0>(r, xoff, j * (8 * DM * 2));
    }
    SCHED_FENCE();
    __syncthreads();
    float ov;
    {
      float mw[NS], lw[NS];
#pragma unroll
      for (int w = 0; w < NS; ++w) { mw[w] = lds[L_ML + 2 * w]; lw[w] = lds[L_ML + 2 * w + 1]; }
      float M = mw[0];
#pragma unroll
      for (int w = 1; w < NS; ++w) M = fmaxf(M, mw[w]);
      float L = 0.f;
#pragma unroll
      for (int w = 0; w < NS; ++w) L = fmaf(__builtin_amdgcn_exp2f(mw[w] - M), lw[w], L);
      const float e0 = __builtin_amdgcn_exp2f(lds[L_ML + 4 * g] - M), e1 = __builtin_amdgcn_exp2f(lds[L_ML + 4 * g + 2] - M);
      ov = e0 * lds[L_PART + (2 * g) * 128 + 16 * a + c] + e1 * lds[L_PART + (2 * g + 1) * 128 + 16 * a + c];
      ov = sum_x16_x32(ov) / L;
      ov = lo16(pack_bf16(ov, 0.f)) * gate;
    }
#pragma unroll
    for (int i = 0; i < 16; ++i) FMA8_HERE(am, rdlane(ov, i), om[i]);
#pragma unroll
    for (int j = 0; j < 2; ++j) FMA8_HERE(ax, __shfl(ov, (lane >> 3) + 8 * j), ox[j]);
#pragma unroll
    for (int e = 0; e < 8; ++e) { float t = ax[e]; t += dpp<XOR8>(t); ax[e] = sum_x16_x32(t); }
    {
      v4f* d = (v4f*)(lds + L_OUT + a * DM + lane * 8);
      d[0] = v4f{am[0], am[1], am[2], am[3]};
      d[1] = v4f{am[4], am[5], am[6], am[7]};
      if (lane < 8) {
        v4f* dx = (v4f*)(lds + L_OUT + a * DM + 512 + lane * 8);
        dx[0] = v4f{ax[0], ax[1], ax[2], ax[3]};
        dx[1] = v4f{ax[4], ax[5], ax[6], ax[7]};
      }
    }
    __syncthreads();
  }
  if (tid < DM) {
    float t = 0.f;
#pragma unroll
    for (int w = 0; w < NA; ++w) t += lds[L_OUT + w * DM + tid];
    part[bid * DM + tid] = t;
  }
}

__global__ void __launch_bounds__(64) final_reduce(const float* __restrict__ part, u16* __restrict__ out) {
  const int b = blockIdx.x / 9, n = (blockIdx.x % 9) * 64 + threadIdx.x;
  float v[NH];
#pragma unroll
  for (int h = 0; h < NH; ++h) v[h] = part[(b * NH + h) * DM + n];
  float t = 0.f;
#pragma unroll
  for (int h = 0; h < NH; ++h) t += v[h];
  out[b * DM + n] = (u16)(pack_bf16(t, 0.f) & 0xffffu);
}

}

extern "C" void kernel_launch(void* const* d_in, const int* in_sizes, int n_in, void* d_out, int out_size,
                              void* d_ws, size_t ws_size, hipStream_t stream) {
  using namespace gm;
  const u16* x = (const u16*)d_in[0];
  const u16* kc = (const u16*)d_in[1];
  const u16* vc = (const u16*)d_in[2];
  const u16* wq = (const u16*)d_in[3];
  const u16* wo = (const u16*)d_in[4];
  const u16* w1 = (const u16*)d_in[6];
  const u16* b1 = (const u16*)d_in[7];
  const u16* w2 = (const u16*)d_in[8];
  const u16* b2 = (const u16*)d_in[9];
  const u16* gw = (const u16*)d_in[10];
  float* part = (float*)d_ws;
  hipLaunchKernelGGL(fused_main, dim3(NB * NH), dim3(NT), 0, stream, x, kc, vc, wq, wo, w1, b1, w2, b2, gw, part);
  hipLaunchKernelGGL(final_reduce, dim3(NB * 9), dim3(64), 0, stream, (const float*)part, (u16*)d_out);
}
```

```cpp
#include <hip/hip_runtime.h>
#include <hip/hip_bf16.h>
#include <stdint.h>

namespace gm {

typedef unsigned v2u __attribute__((ext_vector_type(2)));
typedef unsigned v4u __attribute__((ext_vector_type(4)));
typedef float v2f __attribute__((ext_vector_type(2)));
typedef float v4f __attribute__((ext_vector_type(4)));
typedef __bf16 bf16x2 __attribute__((ext_vector_type(2)));
typedef __bf16 bf16x8 __attribute__((ext_vector_type(8)));
typedef unsigned short u16;
typedef unsigned long long u64;
typedef __amdgpu_buffer_rsrc_t rsrc_t;
#define GAS __attribute__((address_space(1)))
#define LAS __attribute__((address_space(3)))

constexpr int NB = 8, NH = 32, SK = 4096, DH = 128, DM = 576, DF = 4608;
constexpr int NS = 4;
constexpr int NA = 8;
constexpr int NT = (NS + NA) * 64;
constexpr int W = 16;
constexpr int NTL = 2, SC1 = 16;
constexpr unsigned SPIN_CAP = 1u << 18;

constexpr int T_STREAM = 6950, T_OFF = 350;
constexpr int FLY_MID = 24, FLY_MID2 = 21, FLY_N2 = 352, FLY_LO = 20, FLY_HI = 32, FLY_GAIN = 4;
constexpr int T_Q1 = 200, T_QD = 110;
constexpr int T_MLP = 800;
constexpr int T_WO_LATE = 5600;
constexpr int T_POLL = 60;

#define PIN(v) asm volatile("" : "+v"(v))
#define SCHED_FENCE() __builtin_amdgcn_sched_barrier(0)

__device__ __forceinline__ rsrc_t mk_rsrc(const void* p, unsigned bytes) {
  return __builtin_amdgcn_make_buffer_rsrc(const_cast<void*>(p), 0, bytes, 0x00020000);
}
template <int AUX> __device__ __forceinline__ v4u ld16(rsrc_t r, int voff, int soff) {
  return __builtin_amdgcn_raw_buffer_load_b128(r, voff, soff, AUX);
}
template <int AUX> __device__ __forceinline__ unsigned ld4(rsrc_t r, int voff, int soff) {
  return __builtin_amdgcn_raw_buffer_load_b32(r, voff, soff, AUX);
}
__device__ __forceinline__ float lo16(unsigned u) { return __builtin_bit_cast(float, u << 16); }
__device__ __forceinline__ float hi16(unsigned u) { return __builtin_bit_cast(float, u & 0xffff0000u); }
__device__ __forceinline__ float dot2(unsigned a, unsigned b, float acc) {
  return __builtin_amdgcn_fdot2_f32_bf16(__builtin_bit_cast(bf16x2, a), __builtin_bit_cast(bf16x2, b), acc, false);
}
__device__ __forceinline__ unsigned pack_bf16(float lo, float hi) {
  bf16x2 r;
  r[0] = (__bf16)lo;
  r[1] = (__bf16)hi;
  return __builtin_bit_cast(unsigned, r);
}
__device__ __forceinline__ v4f mfma(v4u a, v4u b, v4f c) {
  return __builtin_amdgcn_mfma_f32_16x16x32_bf16(__builtin_bit_cast(bf16x8, a), __builtin_bit_cast(bf16x8, b), c, 0, 0, 0);
}
template <int CTRL> __device__ __forceinline__ unsigned dppu(unsigned x) {
  return (unsigned)__builtin_amdgcn_mov_dpp((int)x, CTRL, 0xf, 0xf, true);
}
template <int CTRL> __device__ __forceinline__ float dpp(float x) {
  return __builtin_bit_cast(float, dppu<CTRL>(__builtin_bit_cast(unsigned, x)));
}
constexpr int XOR1 = 0xB1, XOR2 = 0x4E, XOR7 = 0x141, XOR8 = 0x128, ROR4 = 0x124, BCAST = 0x150;
__device__ __forceinline__ float sum_x16(float x) {
  auto s = __builtin_amdgcn_permlane16_swap(__float_as_uint(x), __float_as_uint(x), false, false);
  return __uint_as_float(s[0]) + __uint_as_float(s[1]);
}
__device__ __forceinline__ float sum_x32(float x) {
  auto t = __builtin_amdgcn_permlane32_swap(__float_as_uint(x), __float_as_uint(x), false, false);
  return __uint_as_float(t[0]) + __uint_as_float(t[1]);
}
__device__ __forceinline__ float sum8(float x) {
  x += dpp<XOR7>(x); x += dpp<XOR2>(x); x += dpp<XOR1>(x);
  return x;
}
__device__ __forceinline__ float wave_sum(float x) { x += dpp<XOR8>(x); return sum_x32(sum_x16(sum8(x))); }
__device__ __forceinline__ float wave_max(float x) {
  x = fmaxf(x, dpp<XOR8>(x)); x = fmaxf(x, dpp<XOR7>(x)); x = fmaxf(x, dpp<XOR2>(x)); x = fmaxf(x, dpp<XOR1>(x));
  auto s = __builtin_amdgcn_permlane16_swap(__float_as_uint(x), __float_as_uint(x), false, false);
  x = fmaxf(__uint_as_float(s[0]), __uint_as_float(s[1]));
  auto t = __builtin_amdgcn_permlane32_swap(__float_as_uint(x), __float_as_uint(x), false, false);
  return fmaxf(__uint_as_float(t[0]), __uint_as_float(t[1]));
}
__device__ __forceinline__ u64 now() { return __builtin_amdgcn_s_memrealtime(); }
__device__ __forceinline__ void sleep_until(u64 t) { while (now() < t) __builtin_amdgcn_s_sleep(2); }
__device__ __forceinline__ void lds_arrive(unsigned* cnt, int lane) {
  __builtin_amdgcn_fence(__ATOMIC_RELEASE, "workgroup");
  if (lane == 0) __hip_atomic_fetch_add(cnt, 1u, __ATOMIC_RELAXED, __HIP_MEMORY_SCOPE_WORKGROUP);
}
template <int SLP> __device__ __forceinline__ void lds_wait(unsigned* cnt, unsigned n) {
  while (__builtin_amdgcn_readfirstlane(__hip_atomic_load(cnt, __ATOMIC_RELAXED, __HIP_MEMORY_SCOPE_WORKGROUP)) < n)
    if (SLP) __builtin_amdgcn_s_sleep(SLP);
  __builtin_amdgcn_fence(__ATOMIC_ACQUIRE, "workgroup");
}
__device__ __forceinline__ u64 next_epoch(u64 z) {
  z += 0x9E3779B97F4A7C15ull;
  z = (z ^ (z >> 30)) * 0xBF58476D1CE4E5B9ull;
  z = (z ^ (z >> 27)) * 0x94D049BB133111EBull;
  return z ^ (z >> 31);
}
__device__ __forceinline__ v4u make_granules(float v0, float v1, unsigned tlo, unsigned thi) {
  return v4u{tlo | ((__builtin_bit_cast(unsigned, v0) + 0x80u) >> 8), thi, tlo | ((__builtin_bit_cast(unsigned, v1) + 0x80u) >> 8), thi};
}
__device__ __forceinline__ bool granules_ok(v4u v, unsigned tlo, unsigned thi) {
  return ((((v[0] ^ tlo) | (v[2] ^ tlo)) & 0xff000000u) | (v[1] ^ thi) | (v[3] ^ thi)) == 0u;
}
__device__ __forceinline__ float granule_val(unsigned lo) { return __builtin_bit_cast(float, lo << 8); }

__device__ __forceinline__ void wait_lim(int lim) {
  asm volatile(
      "s_waitcnt vmcnt(7)\n\t"
      "s_cmp_gt_i32 %0, 7\n\ts_cbranch_scc1 1f\n\ts_waitcnt vmcnt(6)\n\t"
      "s_cmp_gt_i32 %0, 6\n\ts_cbranch_scc1 1f\n\ts_waitcnt vmcnt(5)\n\t"
      "s_cmp_gt_i32 %0, 5\n\ts_cbranch_scc1 1f\n\ts_waitcnt vmcnt(4)\n\t"
      "1:\n" :: "s"(lim) : "scc");
}
__device__ __forceinline__ int pick_lim(unsigned t0, int n0, int rot) {
  const int late = (int)((unsigned)now() - t0) - ((n0 * T_STREAM) >> 9);
  int tot = (n0 >= FLY_N2 ? FLY_MID2 : FLY_MID) + ((late * FLY_GAIN) >> 8);
  tot = tot < FLY_LO ? FLY_LO : (tot > FLY_HI ? FLY_HI : tot);
  return __builtin_amdgcn_readfirstlane((tot + (rot & 3)) >> 2);
}
#define ONE_IN_FLIGHT() do { SCHED_FENCE(); asm volatile("s_waitcnt vmcnt(0)"); SCHED_FENCE(); } while (0)
__device__ __forceinline__ void one_or_two(unsigned el) {
  SCHED_FENCE();
  asm volatile("s_waitcnt vmcnt(1)\n\ts_cmp_ge_u32 %0, %1\n\ts_cbranch_scc1 1f\n\ts_waitcnt vmcnt(0)\n\t1:\n" :: "s"(el), "i"(T_WO_LATE) : "scc");
  SCHED_FENCE();
}
#define CYC(off, step, lo, span) do { off += (step); off = off >= (lo) + (span) ? off - (span) : off; asm volatile("" : "+s"(off)); } while (0)

constexpr int L_Q = 0;
constexpr int L_PART = L_Q + 16 * 128;
constexpr int L_ML = L_PART + NS * 256;
constexpr int L_TAG = L_ML + 8;
constexpr int L_STAT = L_TAG + 4;
constexpr int L_CNT = L_STAT + 4;
constexpr int L_QP = L_CNT + 8;
constexpr int L_HP = L_QP + 64;
constexpr int L_OUT = L_HP + 3 * 16 * 64;
constexpr int OSTR = DM + 4;
constexpr int L_END = L_OUT + 16 * OSTR;
enum { C_HID = 0, C_MLP = 1, C_ML = 2, C_QPART = 3, C_Q = 4, C_STAT = 5 };

constexpr int NCONS = DM / 32;
constexpr int WS_GO = 4096, WS_END = WS_GO + NB * NCONS * NH * 256;

__global__ void __launch_bounds__(NT, 3)
fused_main(const u16* __restrict__ x, const u16* __restrict__ kc, const u16* __restrict__ vc,
           const u16* __restrict__ wq, const u16* __restrict__ wo, const u16* __restrict__ w1,
           const u16* __restrict__ b1, const u16* __restrict__ w2, const u16* __restrict__ b2,
           const u16* __restrict__ gw, unsigned char* ws, u16* __restrict__ out) {
  __shared__ __attribute__((aligned(16))) float lds[L_END];
  const int bid = blockIdx.x, b = bid >> 5, h = bid & 31;
  const int tid = threadIdx.x;
  const int wave = __builtin_amdgcn_readfirstlane(tid >> 6);
  unsigned* cnt = (unsigned*)(lds + L_CNT);

  if (wave >= NS) {
    int lane = tid & 63;
    asm volatile("; helper" : "+v"(lane) : "s"(x), "s"(wq), "s"(gw), "s"(ws), "s"(w1), "s"(w2), "s"(wo) : "memory");
    const rsrc_t rws = mk_rsrc(ws, WS_END);
    __builtin_amdgcn_s_setprio(3);
    const int g = lane >> 4, c = lane & 15;
    const int a = wave - NS;
    const int ht = tid - NS * 64;
    const int s = g + 4 * (c >> 3), e = c & 7;
    unsigned msk[4];
#define AFRAG(rep) v4u{(rep) & msk[0], (rep) & msk[1], (rep) & msk[2], (rep) & msk[3]}
    const bool valid = (g >> 1) == (c >> 3);
    const int dcol = 8 * e + 4 * (g & 1);
    const int part = 2 * a + (c >> 3);
    const u64 tstart = now();
    const rsrc_t rx = mk_rsrc(x + b * DM, DM * 2);
    v4u af[9];
    v4u wb[18];
    unsigned xs[9];
    unsigned pf = 0;
    {
      constexpr int QS = NH * DH * 2;
      const rsrc_t rwq = mk_rsrc(wq, DM * QS);
      const int grp = h >> 3;
      int ir = (lane >> 2) + b;
      ir = ir >= 9 ? ir - 9 : ir;
      const unsigned xown = ld4<NTL>(rx, ir * 128 + (lane & 3) * 4, 16 * a);
      const int vo = s * QS + e * 16, lo = 8 * a * QS + h * (DH * 2);
      int off = lo + b * (64 * QS);
      wb[0] = ld16<0>(rwq, vo, off);
      wb[1] = ld16<0>(rwq, vo + 128, off);
      CYC(off, 64 * QS, lo, DM * QS);
      v4u x0, x1, g0, g1;
      v2u eo;
      if (a == NA - 1) {
        const rsrc_t rg = mk_rsrc(gw, DM * 2);
        x0 = ld16<NTL>(rx, lane * 16, 0); x1 = ld16<NTL>(rx, lane * 16 + 1024, 0);
        g0 = ld16<NTL>(rg, lane * 16, 0); g1 = ld16<NTL>(rg, lane * 16 + 1024, 0);
        eo = __builtin_amdgcn_raw_buffer_load_b64(rws, (lane >> 6) * 8, b * 128, SC1);
      }
      if (ht < 8) cnt[ht] = 0u;
      SCHED_FENCE();
      asm volatile("s_waitcnt lgkmcnt(0)" ::: "memory");
      __builtin_amdgcn_s_barrier();
      SCHED_FENCE();
      const GAS unsigned* wp;
      {
        int hx = ht;
        PIN(hx);
        const int m = (h >> 3) * 8 + b, r9 = hx / 9;
        const char* p1 = (const char*)w1 + (size_t)(18 * m + r9) * (DF * 2) + (h & 7) * 1152 + (hx - 9 * r9) * 128;
        const char* p2 = (const char*)w2 + (size_t)(h & 7) * (576 * DM * 2) + (162 * m + hx - 162) * 128;
        const char* p3 = (const char*)wo + (size_t)h * (DH * DM * 2) + (144 * b + (hx < 468 ? hx : 467) - 324) * 128;
        wp = (const GAS unsigned*)(hx < 162 ? p1 : (hx < 324 ? p2 : p3));
      }
      if (grp != 0) {
        if (b == 7) {
          wb[2] = ld16<0>(rwq, vo, off); wb[3] = ld16<0>(rwq, vo + 128, off);
          asm volatile("" :: "v"(wb[2]), "v"(wb[3]));
        }
        SCHED_FENCE();
        pf = *wp;
        SCHED_FENCE();
        sleep_until(tstart + T_Q1 + (grp - 1) * T_QD);
      }
      SCHED_FENCE();
#pragma unroll
      for (int i = 1; i < 9; ++i) {
        wb[2 * i] = ld16<0>(rwq, vo, off);
        wb[2 * i + 1] = ld16<0>(rwq, vo + 128, off);
        CYC(off, 64 * QS, lo, DM * QS);
      }
      SCHED_FENCE();
      {
        const unsigned hm = (e & 1) ? 0xffff0000u : 0x0000ffffu;
#pragma unroll
        for (int r = 0; r < 4; ++r) msk[r] = (e >> 1) == r ? hm : 0u;
      }
      const unsigned rsel = (s & 1) ? 0x03020302u : 0x01000100u;
#pragma unroll
      for (int i = 0; i < 9; ++i) {
        const unsigned pr = (unsigned)__builtin_amdgcn_ds_bpermute((4 * i + (s >> 1)) * 4, (int)xown);
        xs[i] = __builtin_amdgcn_perm(pr, pr, rsel);
        af[i] = AFRAG(xs[i]);
        PIN(af[i]);
      }
      SCHED_FENCE();
      v4f q0 = {0.f, 0.f, 0.f, 0.f}, q1 = {0.f, 0.f, 0.f, 0.f};
#pragma unroll
      for (int i = 0; i < 9; ++i) { q0 = mfma(af[i], wb[2 * i], q0); q1 = mfma(af[i], wb[2 * i + 1], q1); SCHED_FENCE(); }
      if (valid) {
        *(v4f*)(lds + L_Q + part * 128 + dcol) = q0;
        *(v4f*)(lds + L_Q + part * 128 + 64 + dcol) = q1;
      }
      lds_arrive(cnt + C_QPART, lane);
      if (grp == 0) { SCHED_FENCE(); pf = *wp; SCHED_FENCE(); }
      if (a == 0) {
        lds_wait<0>(cnt + C_QPART, NA);
        v2f t = *(const v2f*)(lds + L_Q + 2 * lane);
#pragma unroll
        for (int p = 1; p < 16; ++p) t += *(const v2f*)(lds + L_Q + p * 128 + 2 * lane);
        *(unsigned*)(lds + L_QP + lane) = pack_bf16(t[0], t[1]);
        lds_arrive(cnt + C_Q, lane);
      }
      __builtin_amdgcn_s_setprio(0);
      if (a == NA - 1) {
        PIN(eo); PIN(x0); PIN(x1); PIN(g0); PIN(g1);
        float s1 = 0.f, gd = 0.f;
#pragma unroll
        for (int i = 0; i < 4; ++i) {
          const unsigned u0 = x0[i], u1 = x1[i], w0 = g0[i], w1_ = g1[i];
          s1 += (lo16(u0) + hi16(u0)) + (lo16(u1) + hi16(u1));
          gd = dot2(u1, w1_, dot2(u0, w0, gd));
        }
        s1 = wave_sum(s1);
        gd = wave_sum(gd);
        const float mean = s1 * (1.0f / DM);
        float s2 = 0.f, s3 = 0.f;
#pragma unroll
        for (int i = 0; i < 4; ++i) {
          const unsigned u0 = x0[i], u1 = x1[i];
          const float v0 = lo16(u0) - mean, v1 = hi16(u0) - mean, v2 = lo16(u1) - mean, v3 = hi16(u1) - mean;
          s2 = fmaf(v0, v0, fmaf(v1, v1, s2));
          s3 = fmaf(v2, v2, fmaf(v3, v3, s3));
        }
        s2 += lane < 8 ? s3 : 0.f;
        s2 = wave_sum(s2);
        if (lane == 0) {
          *(v4f*)(lds + L_STAT) = v4f{mean, 1.0f / sqrtf(s2 * (1.0f / DM) + 1e-5f), 1.0f / (1.0f + __expf(-gd)), 0.f};
          *(u64*)(lds + L_TAG) = next_epoch(__builtin_bit_cast(u64, eo));
        }
        lds_arrive(cnt + C_STAT, lane);
      }
      SCHED_FENCE();
    }
    float gate;
    unsigned tlo, thi;
    {
      lds_wait<2>(cnt + C_STAT, 1);
      const v4f st = *(const v4f*)(lds + L_STAT);
      const u64 z = *(const u64*)(lds + L_TAG);
      gate = st[2];
#pragma unroll
      for (int i = 0; i < 9; ++i) {
        const float y = (lo16(xs[i]) - st[0]) * st[1];
        const unsigned rep = pack_bf16(y, y);
        af[i] = AFRAG(rep);
        PIN(af[i]);
      }
      tlo = (unsigned)z & 0xff000000u;
      thi = (unsigned)(z >> 32);
    }
    sleep_until(tstart + T_MLP);
    asm volatile("" :: "v"(pf));
    const int nu = (h >> 3) == 3 ? 3 : 2;
    const int f0 = (h & 7) * 576 + (h >> 3) * 128;
    __builtin_amdgcn_s_setprio(3);
    {
      const rsrc_t r1 = mk_rsrc(w1, DM * DF * 2);
      const int vo = s * (DF * 2) + e * 16;
#pragma unroll 1
      for (int u = 0; u < nu; ++u) {
        const int lo = 8 * a * (DF * 2) + f0 * 2 + u * 128;
        int off = lo + b * (64 * DF * 2);
#pragma unroll
        for (int i = 0; i < 9; ++i) { ONE_IN_FLIGHT(); wb[i] = ld16<0>(r1, vo, off); CYC(off, 64 * DF * 2, lo, DM * DF * 2); }
        SCHED_FENCE();
        v4f d = {0.f, 0.f, 0.f, 0.f};
#pragma unroll
        for (int i = 0; i < 9; ++i) { d = mfma(af[i], wb[i], d); SCHED_FENCE(); }
        if (valid) *(v4f*)(lds + L_HP + (u * 16 + part) * 64 + dcol) = d;
      }
    }
    lds_arrive(cnt + C_HID, lane);
    const int vo2 = s * (DM * 2) + e * 16;
    {
      v4f am[9];
#pragma unroll
      for (int t = 0; t < 9; ++t) am[t] = v4f{0.f, 0.f, 0.f, 0.f};
      const rsrc_t r2 = mk_rsrc(w2 + (size_t)f0 * DM, nu * 64 * DM * 2);
      const rsrc_t rb1 = mk_rsrc(b1 + f0, nu * 128);
#pragma unroll 1
      for (int u = 0; u < nu; ++u) {
        const unsigned bias = __builtin_amdgcn_raw_buffer_load_b16(rb1, s * 2, u * 128 + a * 16, NTL);
#pragma unroll
        for (int t = 0; t < 9; ++t) { if (t) ONE_IN_FLIGHT(); wb[t] = ld16<0>(r2, vo2, (u * 64 + 8 * a) * (DM * 2) + t * 128); }
        SCHED_FENCE();
        lds_wait<1>(cnt + C_HID, NA);
        const float* hp = lds + L_HP + (u * 16 + 2 * e) * 64 + 8 * a + s;
        float hv = sum8(hp[0] + hp[64]) + lo16(bias);
        const float uu = 0.7978845608028654f * (hv + 0.044715f * hv * hv * hv);
        hv = hv / (1.0f + __expf(-2.0f * uu));
        const unsigned rep = pack_bf16(hv, hv);
        const v4u ah = AFRAG(rep);
#pragma unroll
        for (int t = 0; t < 9; ++t) { am[t] = mfma(ah, wb[t], am[t]); SCHED_FENCE(); }
      }
      if (valid) {
#pragma unroll
        for (int t = 0; t < 9; ++t) *(v4f*)(lds + L_OUT + part * OSTR + dcol + t * 64) = am[t];
      }
    }
    lds_arrive(cnt + C_MLP, lane);
    const unsigned bb = ld4<NTL>(mk_rsrc(b2, h == 0 ? DM * 2 : 0), ht * 4, 0);
    {
      const rsrc_t r3 = mk_rsrc(wo + (size_t)h * DH * DM, DH * DM * 2);
      const unsigned el = (unsigned)(now() - tstart);
      const int vo3 = g * (DM * 2) + c * 16;
#pragma unroll
      for (int q = 0; q < 4; ++q)
#pragma unroll
        for (int t = 0; t < 4; ++t) { one_or_two(el); wb[4 * q + t] = ld16<0>(r3, vo3, (16 * a + 4 * q) * (DM * 2) + t * 256); }
#pragma unroll
      for (int k = 0; k < 2; ++k) { one_or_two(el); wb[16 + k] = ld16<0>(r3, vo2, (16 * a + 8 * k) * (DM * 2) + 1024); }
    }
    SCHED_FENCE();
    __builtin_amdgcn_s_setprio(0);
    const int tq = ht < 288 ? ht : 287;
    v2f mlp = v2f{lo16(bb), hi16(bb)};
    lds_wait<1>(cnt + C_MLP, NA);
#pragma unroll
    for (int p = 0; p < 16; ++p) mlp += *(const v2f*)(lds + L_OUT + p * OSTR + 2 * tq);
    mlp *= 1.0f - gate;
    PIN(mlp);
#pragma unroll
    for (int i = 0; i < 18; ++i) PIN(wb[i]);
    float fac;
    {
      lds_wait<16>(cnt + C_ML, NS);
      const v4f m01 = *(const v4f*)(lds + L_ML), m23 = *(const v4f*)(lds + L_ML + 4);
      const float M = fmaxf(fmaxf(m01[0], m01[2]), fmaxf(m23[0], m23[2]));
      float L = __builtin_amdgcn_exp2f(m01[0] - M) * m01[1];
      L = fmaf(__builtin_amdgcn_exp2f(m01[2] - M), m01[3], L);
      L = fmaf(__builtin_amdgcn_exp2f(m23[0] - M), m23[1], L);
      L = fmaf(__builtin_amdgcn_exp2f(m23[2] - M), m23[3], L);
      fac = __builtin_amdgcn_exp2f(lds[L_ML + (e >> 1) * 2] - M) / L;
      PIN(fac);
    }
    int gvo = (ht >> 4) * (NH * 256) + (ht & 15) * 16;
    unsigned mlo[4], mhi[4];
#pragma unroll
    for (int r = 0; r < 4; ++r) { mlo[r] = c < 8 ? msk[r] : 0u; mhi[r] = c < 8 ? 0u : msk[r]; PIN(mlo[r]); PIN(mhi[r]); }
    LAS float* l3 = (LAS float*)lds;
    LAS float* pp = l3 + L_PART + e * 128 + 16 * a + 8 * (c >> 3) + g;
    LAS v4f* ow = (LAS v4f*)(l3 + L_OUT + a * OSTR + 128 * (g >> 1) + 8 * c + 4 * (g & 1));
    LAS v4f* o8 = (LAS v4f*)(l3 + (valid ? L_OUT + part * OSTR + dcol + 512 : L_HP + lane * 4));
    LAS v2f* rd = (LAS v2f*)(l3 + L_OUT + 2 * tq);
    asm volatile("" : "+v"(gvo), "+v"(pp), "+v"(ow), "+v"(o8), "+v"(rd), "+v"(tlo), "+v"(thi));
    __builtin_amdgcn_s_setprio(3);
    SCHED_FENCE();
    __syncthreads();
    {
      float t0 = pp[0] * fac, t1 = pp[4] * fac;
      PIN(t0); PIN(t1);
      const unsigned own = pack_bf16(sum8(t0), sum8(t1));
      const unsigned oth = dppu<XOR8>(own);
      const unsigned p01 = c < 8 ? own : oth, p23 = c < 8 ? oth : own;
      const unsigned rq[4] = {__builtin_amdgcn_perm(p01, p01, 0x01000100u), __builtin_amdgcn_perm(p01, p01, 0x03020302u),
                              __builtin_amdgcn_perm(p23, p23, 0x01000100u), __builtin_amdgcn_perm(p23, p23, 0x03020302u)};
      v4u fl[4], fh[4];
#pragma unroll
      for (int q = 0; q < 4; ++q) {
        fl[q] = v4u{rq[q] & mlo[0], rq[q] & mlo[1], rq[q] & mlo[2], rq[q] & mlo[3]};
        fh[q] = v4u{rq[q] & mhi[0], rq[q] & mhi[1], rq[q] & mhi[2], rq[q] & mhi[3]};
      }
      v4f d0 = {0.f, 0.f, 0.f, 0.f}, d1 = {0.f, 0.f, 0.f, 0.f};
#pragma unroll
      for (int q = 0; q < 4; ++q) { d0 = mfma(fl[q], wb[4 * q], d0); d0 = mfma(fh[q], wb[4 * q + 1], d0); }
      const v4f d8 = mfma(fl[2] | fh[3], wb[17], mfma(fl[0] | fh[1], wb[16], v4f{0.f, 0.f, 0.f, 0.f}));
      ow[0] = d0;
#pragma unroll
      for (int q = 0; q < 4; ++q) { d1 = mfma(fl[q], wb[4 * q + 2], d1); d1 = mfma(fh[q], wb[4 * q + 3], d1); }
      *o8 = d8;
      ow[64] = d1;
    }
    __syncthreads();
    if (a < 5) {
      constexpr int RS = OSTR / 2;
      v2f t = ((rd[0] + rd[RS]) + (rd[2 * RS] + rd[3 * RS])) + ((rd[4 * RS] + rd[5 * RS]) + (rd[6 * RS] + rd[7 * RS]));
      if (a == 4) t += ((rd[8 * RS] + rd[9 * RS]) + (rd[10 * RS] + rd[11 * RS])) + ((rd[12 * RS] + rd[13 * RS]) + (rd[14 * RS] + rd[15 * RS]));
      const v4u gr = make_granules(fmaf(gate, t[0], mlp[0]), fmaf(gate, t[1], mlp[1]), tlo, thi);
      if (ht < 288) __builtin_amdgcn_raw_buffer_store_b128(gr, rws, gvo, WS_GO + b * (NCONS * NH * 256) + h * 256, SC1);
    }
  } else {
    int lane = tid & 63;
    asm volatile("; streaming" : "+v"(lane) : "s"(kc) : "memory");
    __builtin_amdgcn_s_setprio(2);
    const int sw = wave;
    const int c = lane & 15;
    constexpr int HB = SK * DH * 2;
    const rsrc_t rk = mk_rsrc(kc + (size_t)bid * (HB / 2), HB);
    const rsrc_t rv = mk_rsrc(vc + (size_t)bid * (HB / 2), HB);
    const int vo = lane * 16 + sw * 1024;
    constexpr int LS = NS * 1024;
    v4u buf[W], pre[W];
    SCHED_FENCE();
#pragma unroll
    for (int j = 0; j < 4; ++j) pre[j] = ld16<NTL>(rk, vo, j * LS);
    SCHED_FENCE();
    const unsigned t0 = (unsigned)now() + T_OFF;
    SCHED_FENCE();
    __builtin_amdgcn_s_barrier();
    SCHED_FENCE();
#pragma unroll
    for (int j = 4; j < W; ++j) { pre[j] = ld16<NTL>(rk, vo, j * LS); SCHED_FENCE(); }
#pragma unroll
    for (int j = 0; j < W; ++j) { buf[j] = ld16<NTL>(rk, vo, W * LS + j * LS); SCHED_FENCE(); }
    lds_wait<0>(cnt + C_Q, 1);
    const v4u qp = *(const v4u*)(lds + L_QP + 4 * c);
    constexpr int NBLK = SK / NS / 4 / W;
    float s[NBLK];
#pragma unroll
    for (int i = 0; i < NBLK; ++i) s[i] = 0.f;
#define TR_STEP(xs, HALF, CTRL)                                     \
  _Pragma("unroll") for (int u = 0; u < (HALF); ++u) {              \
    const float lo_ = xs[u], hi_ = xs[u + (HALF)];                  \
    const float t1_ = lo_ + dpp<CTRL>(lo_), t2_ = hi_ + dpp<CTRL>(hi_); \
    xs[u] = (c & (HALF)) ? t2_ : t1_;                               \
  }
#define TR(xs) TR_STEP(xs, 8, XOR8) TR_STEP(xs, 4, XOR7) TR_STEP(xs, 2, XOR2) TR_STEP(xs, 1, XOR1)
#define K_DOT(cur, dst)                                             \
  {                                                                 \
    float d = dot2(cur[0], qp[0], 0.f);                             \
    d = dot2(cur[1], qp[1], d);                                     \
    d = dot2(cur[2], qp[2], d);                                     \
    d = dot2(cur[3], qp[3], d);                                     \
    PIN(d);                                                         \
    dst = d;                                                        \
  }
    {
      float xs[W], x0[W];
      int lim = 0;
#pragma unroll
      for (int j = 0; j < W; ++j) {
        if ((j & 7) == 0) lim = pick_lim(t0, 2 * W + j, sw + (j >> 3));
        const v4u cur = buf[j];
        K_DOT(cur, xs[j])
        wait_lim(lim);
        buf[j] = ld16<NTL>(rk, vo, 2 * W * LS + j * LS);
        SCHED_FENCE();
        const v4u old = pre[j];
        K_DOT(old, x0[j])
        SCHED_FENCE();
      }
      TR(xs)
      TR(x0)
      s[0] = x0[0];
      s[1] = xs[0];
    }
#pragma unroll 1
    for (int blk = 2; blk < NBLK; ++blk) {
      const bool last = blk + 1 == NBLK;
      const rsrc_t rn = last ? rv : rk;
      const int so = last ? 0 : (blk + 1) * (W * LS);
      float xs[W];
      int lim = 0;
#pragma unroll
      for (int j = 0; j < W; ++j) {
        if ((j & 7) == 0) lim = pick_lim(t0, (blk + 1) * W + j, sw + 2 * blk + (j >> 3));
        const v4u cur = buf[j];
        K_DOT(cur, xs[j])
        wait_lim(lim);
        buf[j] = ld16<NTL>(rn, vo, so + j * LS);
        SCHED_FENCE();
      }
      TR(xs)
#pragma unroll
      for (int i = 2; i + 1 < NBLK; ++i) s[i] = s[i + 1];
      s[NBLK - 1] = xs[0];
    }
#undef TR
#undef TR_STEP
#undef K_DOT
    constexpr float C2 = 0.088388347648318440f * 1.4426950408889634f;
    unsigned pp[NBLK];
    {
      float m = s[0];
#pragma unroll
      for (int i = 1; i < NBLK; ++i) m = fmaxf(m, s[i]);
      m = wave_max(m);
      float l = 0.f;
#pragma unroll
      for (int i = 0; i < NBLK; ++i) {
        const float p = __builtin_amdgcn_exp2f((s[i] - m) * C2);
        l += p;
        const float ph = hi16(__builtin_bit_cast(unsigned, p));
        pp[i] = pack_bf16(ph, p - ph);
      }
      l = wave_sum(l);
      if (lane == 0) *(v2f*)(lds + L_ML + 2 * sw) = v2f{m * C2, l};
      lds_arrive(cnt + C_ML, lane);
    }
    unsigned sel[4];
    {
      const unsigned src = (c & 8) ? 0x0302u : 0x0100u;
      const unsigned pl = (c & 1) ? (src << 16) | 0x0c0cu : 0x0c0c0000u | src;
#pragma unroll
      for (int r = 0; r < 4; ++r) sel[r] = ((c & 7) >> 1) == r ? pl : 0x0c0c0c0cu;
    }
    v4f acc = {0.f, 0.f, 0.f, 0.f};
#define V_STEP(J, REFILL)                                           \
  {                                                                 \
    const unsigned pb = dppu<BCAST + (J)>(p);                       \
    v4u af;                                                         \
    _Pragma("unroll") for (int r = 0; r < 4; ++r) af[r] = __builtin_amdgcn_perm(pb, pb, sel[r]); \
    acc = mfma(af, buf[J], acc);                                    \
    PIN(acc);                                                       \
    if (REFILL) {                                                   \
      if (((J) & 7) == 0) lim = pick_lim(t0, (NBLK + blk + 1) * W + (J), sw + 2 * blk + ((J) >> 3)); \
      wait_lim(lim);                                                \
      buf[J] = ld16<NTL>(rv, vo, so + (J) * LS);                    \
    }                                                               \
    SCHED_FENCE();                                                  \
  }
#define V_BLOCK(REFILL)                                             \
  V_STEP(0, REFILL) V_STEP(1, REFILL) V_STEP(2, REFILL) V_STEP(3, REFILL)     \
  V_STEP(4, REFILL) V_STEP(5, REFILL) V_STEP(6, REFILL) V_STEP(7, REFILL)     \
  V_STEP(8, REFILL) V_STEP(9, REFILL) V_STEP(10, REFILL) V_STEP(11, REFILL)   \
  V_STEP(12, REFILL) V_STEP(13, REFILL) V_STEP(14, REFILL) V_STEP(15, REFILL)
#pragma unroll 1
    for (int blk = 0; blk + 1 < NBLK; ++blk) {
      const int so = (blk + 1) * (W * LS);
      const unsigned p = pp[0];
      int lim = 0;
      V_BLOCK(true)
#pragma unroll
      for (int i = 0; i + 1 < NBLK; ++i) pp[i] = pp[i + 1];
    }
    {
      const int so = 0, blk = 0;
      const unsigned p = pp[0];
      int lim = 0;
      V_BLOCK(false)
    }
#undef V_BLOCK
#undef V_STEP
    *(v4f*)(lds + L_PART + sw * 256 + (lane >> 5) * 128 + c * 8 + ((lane >> 4) & 1) * 4) = acc;
    __syncthreads();
    const rsrc_t rws = mk_rsrc(ws, WS_END);
    const u64 z = *(const u64*)(lds + L_TAG);
    int go = (lane >> 2) * 256 + (lane & 3) * 16;
    GAS unsigned* op = (GAS unsigned*)(out + b * DM + 32 * h + 8 * sw) + (lane & 3);
    asm volatile("" : "+v"(go), "+v"(op));
    __builtin_amdgcn_s_barrier();
    if (h < NCONS) {
      const unsigned tlo = (unsigned)z & 0xff000000u, thi = (unsigned)(z >> 32);
      const int so = WS_GO + (b * NCONS + h) * (NH * 256) + sw * 64;
      sleep_until(now() + T_POLL);
      v4u v0, v1;
      for (unsigned spins = 0;; ++spins) {
        v0 = ld16<SC1>(rws, go, so);
        v1 = ld16<SC1>(rws, go + 4096, so);
        if (__all(granules_ok(v0, tlo, thi) & granules_ok(v1, tlo, thi))) break;
        if (spins > SPIN_CAP) { v0[0] = v0[2] = 0x7fc000u; break; }
        asm volatile("" ::: "memory");
      }
      const unsigned a0 = v0[0], a1 = v0[2], b0 = v1[0], b1_ = v1[2];
      float r0 = granule_val(a0) + granule_val(b0), r1 = granule_val(a1) + granule_val(b1_);
      r0 += dpp<ROR4>(r0); r1 += dpp<ROR4>(r1);
      r0 += dpp<XOR8>(r0); r1 += dpp<XOR8>(r1);
      r0 = sum_x32(sum_x16(r0));
      r1 = sum_x32(sum_x16(r1));
      if (lane < 4) *op = pack_bf16(r0, r1);
      if (h == 0 && sw == 0 && lane == 0) __hip_atomic_store((GAS u64*)(ws + b * 128), z, __ATOMIC_RELAXED, __HIP_MEMORY_SCOPE_AGENT);
    }
  }
}

}

extern "C" void kernel_launch(void* const* d_in, const int* in_sizes, int n_in, void* d_out, int out_size,
                              void* d_ws, size_t ws_size, hipStream_t stream) {
  using namespace gm;
  const u16* x = (const u16*)d_in[0];
  const u16* kc = (const u16*)d_in[1];
  const u16* vc = (const u16*)d_in[2];
  const u16* wq = (const u16*)d_in[3];
  const u16* wo = (const u16*)d_in[4];
  const u16* w1 = (const u16*)d_in[6];
  const u16* b1 = (const u16*)d_in[7];
  const u16* w2 = (const u16*)d_in[8];
  const u16* b2 = (const u16*)d_in[9];
  const u16* gw = (const u16*)d_in[10];
  hipLaunchKernelGGL(fused_main, dim3(NB * NH), dim3(NT), 0, stream, x, kc, vc, wq, wo, w1, b1, w2, b2, gw, (unsigned char*)d_ws, (u16*)d_out);
}
```

```cpp
#include <hip/hip_runtime.h>
#include <hip/hip_bf16.h>
#include <stdint.h>

namespace gm {

typedef unsigned v2u __attribute__((ext_vector_type(2)));
typedef unsigned v4u __attribute__((ext_vector_type(4)));
typedef float v2f __attribute__((ext_vector_type(2)));
typedef float v4f __attribute__((ext_vector_type(4)));
typedef __bf16 bf16x2 __attribute__((ext_vector_type(2)));
typedef __bf16 bf16x8 __attribute__((ext_vector_type(8)));
typedef unsigned short u16;
typedef unsigned long long u64;
typedef __amdgpu_buffer_rsrc_t rsrc_t;
#define GAS __attribute__((address_space(1)))
#define LAS __attribute__((address_space(3)))

constexpr int NB = 8, NH = 32, SK = 4096, DH = 128, DM = 576, DF = 4608;
constexpr int NS = 4;
constexpr int NA = 8;
constexpr int NT = (NS + NA) * 64;
constexpr int W = 16;
constexpr int NTL = 2, SC1 = 16;
constexpr unsigned SPIN_CAP = 1u << 18;

constexpr int T_STREAM = 6950, T_OFF = 330;
constexpr int FLY_MID = 24, FLY_MID2 = 21, FLY_N2 = 352, FLY_LO = 20, FLY_HI = 32, FLY_GAIN = 4;
constexpr int T_Q1 = 180, T_QD = 115;
constexpr int T_MLP = 900;
constexpr int T_WLOAD = 80, T_WEND = 7000;
constexpr int T_POLL = 60;

#define PIN(v) asm volatile("" : "+v"(v))
#define SCHED_FENCE() __builtin_amdgcn_sched_barrier(0)

__device__ __forceinline__ rsrc_t mk_rsrc(const void* p, unsigned bytes) {
  return __builtin_amdgcn_make_buffer_rsrc(const_cast<void*>(p), 0, bytes, 0x00020000);
}
template <int AUX> __device__ __forceinline__ v4u ld16(rsrc_t r, int voff, int soff) {
  return __builtin_amdgcn_raw_buffer_load_b128(r, voff, soff, AUX);
}
template <int AUX> __device__ __forceinline__ unsigned ld4(rsrc_t r, int voff, int soff) {
  return __builtin_amdgcn_raw_buffer_load_b32(r, voff, soff, AUX);
}
__device__ __forceinline__ float lo16(unsigned u) { return __builtin_bit_cast(float, u << 16); }
__device__ __forceinline__ float hi16(unsigned u) { return __builtin_bit_cast(float, u & 0xffff0000u); }
__device__ __forceinline__ float dot2(unsigned a, unsigned b, float acc) {
  return __builtin_amdgcn_fdot2_f32_bf16(__builtin_bit_cast(bf16x2, a), __builtin_bit_cast(bf16x2, b), acc, false);
}
__device__ __forceinline__ unsigned pack_bf16(float lo, float hi) {
  bf16x2 r;
  r[0] = (__bf16)lo;
  r[1] = (__bf16)hi;
  return __builtin_bit_cast(unsigned, r);
}
__device__ __forceinline__ v4f mfma(v4u a, v4u b, v4f c) {
  return __builtin_amdgcn_mfma_f32_16x16x32_bf16(__builtin_bit_cast(bf16x8, a), __builtin_bit_cast(bf16x8, b), c, 0, 0, 0);
}
template <int CTRL> __device__ __forceinline__ unsigned dppu(unsigned x) {
  return (unsigned)__builtin_amdgcn_mov_dpp((int)x, CTRL, 0xf, 0xf, true);
}
template <int CTRL> __device__ __forceinline__ float dpp(float x) {
  return __builtin_bit_cast(float, dppu<CTRL>(__builtin_bit_cast(unsigned, x)));
}
constexpr int XOR1 = 0xB1, XOR2 = 0x4E, XOR7 = 0x141, XOR8 = 0x128, ROR4 = 0x124, BCAST = 0x150;
__device__ __forceinline__ float sum_x16(float x) {
  auto s = __builtin_amdgcn_permlane16_swap(__float_as_uint(x), __float_as_uint(x), false, false);
  return __uint_as_float(s[0]) + __uint_as_float(s[1]);
}
__device__ __forceinline__ float sum_x32(float x) {
  auto t = __builtin_amdgcn_permlane32_swap(__float_as_uint(x), __float_as_uint(x), false, false);
  return __uint_as_float(t[0]) + __uint_as_float(t[1]);
}
__device__ __forceinline__ float sum8(float x) {
  x += dpp<XOR7>(x); x += dpp<XOR2>(x); x += dpp<XOR1>(x);
  return x;
}
__device__ __forceinline__ float wave_sum(float x) { x += dpp<XOR8>(x); return sum_x32(sum_x16(sum8(x))); }
__device__ __forceinline__ float wave_max(float x) {
  x = fmaxf(x, dpp<XOR8>(x)); x = fmaxf(x, dpp<XOR7>(x)); x = fmaxf(x, dpp<XOR2>(x)); x = fmaxf(x, dpp<XOR1>(x));
  auto s = __builtin_amdgcn_permlane16_swap(__float_as_uint(x), __float_as_uint(x), false, false);
  x = fmaxf(__uint_as_float(s[0]), __uint_as_float(s[1]));
  auto t = __builtin_amdgcn_permlane32_swap(__float_as_uint(x), __float_as_uint(x), false, false);
  return fmaxf(__uint_as_float(t[0]), __uint_as_float(t[1]));
}
__device__ __forceinline__ u64 now() { return __builtin_amdgcn_s_memrealtime(); }
__device__ __forceinline__ void sleep_until(u64 t) { while (now() < t) __builtin_amdgcn_s_sleep(2); }
__device__ __forceinline__ void lds_arrive(unsigned* cnt, int lane) {
  __builtin_amdgcn_fence(__ATOMIC_RELEASE, "workgroup");
  if (lane == 0) __hip_atomic_fetch_add(cnt, 1u, __ATOMIC_RELAXED, __HIP_MEMORY_SCOPE_WORKGROUP);
}
template <int SLP> __device__ __forceinline__ void lds_wait(unsigned* cnt, unsigned n) {
  while (__builtin_amdgcn_readfirstlane(__hip_atomic_load(cnt, __ATOMIC_RELAXED, __HIP_MEMORY_SCOPE_WORKGROUP)) < n)
    if (SLP) __builtin_amdgcn_s_sleep(SLP);
  __builtin_amdgcn_fence(__ATOMIC_ACQUIRE, "workgroup");
}
__device__ __forceinline__ u64 next_epoch(u64 z) {
  z += 0x9E3779B97F4A7C15ull;
  z = (z ^ (z >> 30)) * 0xBF58476D1CE4E5B9ull;
  z = (z ^ (z >> 27)) * 0x94D049BB133111EBull;
  return z ^ (z >> 31);
}
__device__ __forceinline__ v4u make_granules(float v0, float v1, unsigned tlo, unsigned thi) {
  return v4u{tlo | ((__builtin_bit_cast(unsigned, v0) + 0x80u) >> 8), thi, tlo | ((__builtin_bit_cast(unsigned, v1) + 0x80u) >> 8), thi};
}
__device__ __forceinline__ bool granules_ok(v4u v, unsigned tlo, unsigned thi) {
  return ((((v[0] ^ tlo) | (v[2] ^ tlo)) & 0xff000000u) | (v[1] ^ thi) | (v[3] ^ thi)) == 0u;
}
__device__ __forceinline__ float granule_val(unsigned lo) { return __builtin_bit_cast(float, lo << 8); }

__device__ __forceinline__ void wait_lim(int lim) {
  asm volatile(
      "s_waitcnt vmcnt(7)\n\t"
      "s_cmp_gt_i32 %0, 7\n\ts_cbranch_scc1 1f\n\ts_waitcnt vmcnt(6)\n\t"
      "s_cmp_gt_i32 %0, 6\n\ts_cbranch_scc1 1f\n\ts_waitcnt vmcnt(5)\n\t"
      "s_cmp_gt_i32 %0, 5\n\ts_cbranch_scc1 1f\n\ts_waitcnt vmcnt(4)\n\t"
      "1:\n" :: "s"(lim) : "scc");
}
__device__ __forceinline__ int pick_lim(unsigned t0, int n0, int rot) {
  const int late = (int)((unsigned)now() - t0) - ((n0 * T_STREAM) >> 9);
  int tot = (n0 >= FLY_N2 ? FLY_MID2 : FLY_MID) + ((late * FLY_GAIN) >> 8);
  tot = tot < FLY_LO ? FLY_LO : (tot > FLY_HI ? FLY_HI : tot);
  return __builtin_amdgcn_readfirstlane((tot + (rot & 3)) >> 2);
}
__device__ __forceinline__ void one_or_two(unsigned eta) {
  SCHED_FENCE();
  asm volatile("s_waitcnt vmcnt(1)\n\ts_cmp_ge_u32 %0, %1\n\ts_cbranch_scc1 1f\n\ts_waitcnt vmcnt(0)\n\t1:\n" :: "s"(eta), "i"(T_WEND) : "scc");
  SCHED_FENCE();
}
#define CYC(off, step, lo, span) do { off += (step); off = off >= (lo) + (span) ? off - (span) : off; asm volatile("" : "+s"(off)); } while (0)

constexpr int L_Q = 0;
constexpr int L_PART = L_Q + 16 * 128;
constexpr int L_ML = L_PART + NS * 256;
constexpr int L_TAG = L_ML + 8;
constexpr int L_STAT = L_TAG + 4;
constexpr int L_CNT = L_STAT + 4;
constexpr int L_QP = L_CNT + 8;
constexpr int L_HP = L_QP + 64;
constexpr int L_OUT = L_HP + 3 * 16 * 64;
constexpr int OSTR = DM + 4;
constexpr int L_END = L_OUT + 16 * OSTR;
enum { C_HID = 0, C_MLP = 1, C_ML = 2, C_QPART = 3, C_Q = 4, C_STAT = 5 };

constexpr int NCONS = DM / 32;
constexpr int WS_GO = 4096, WS_END = WS_GO + NB * NCONS * NH * 256;

__global__ void __launch_bounds__(NT, 3)
fused_main(const u16* __restrict__ x, const u16* __restrict__ kc, const u16* __restrict__ vc,
           const u16* __restrict__ wq, const u16* __restrict__ wo, const u16* __restrict__ w1,
           const u16* __restrict__ b1, const u16* __restrict__ w2, const u16* __restrict__ b2,
           const u16* __restrict__ gw, unsigned char* ws, u16* __restrict__ out) {
  __shared__ __attribute__((aligned(16))) float lds[L_END];
  const int bid = blockIdx.x, b = bid >> 5, h = bid & 31;
  const int tid = threadIdx.x;
  const int wave = __builtin_amdgcn_readfirstlane(tid >> 6);
  unsigned* cnt = (unsigned*)(lds + L_CNT);

  if (wave >= NS) {
    int lane = tid & 63;
    asm volatile("; helper" : "+v"(lane) : "s"(x), "s"(wq), "s"(gw), "s"(ws), "s"(w1), "s"(w2), "s"(wo) : "memory");
    const rsrc_t rws = mk_rsrc(ws, WS_END);
    __builtin_amdgcn_s_setprio(3);
    const int g = lane >> 4, c = lane & 15;
    const int a = wave - NS;
    const int ht = tid - NS * 64;
    const int s = g + 4 * (c >> 3), e = c & 7;
    unsigned msk[4];
#define AFRAG(rep) v4u{(rep) & msk[0], (rep) & msk[1], (rep) & msk[2], (rep) & msk[3]}
    const bool valid = (g >> 1) == (c >> 3);
    const int dcol = 8 * e + 4 * (g & 1);
    const int part = 2 * a + (c >> 3);
    u64 tstart;
    const rsrc_t rx = mk_rsrc(x + b * DM, DM * 2);
    v4u af[9];
    v4u wb[18];
    unsigned xs[9];
    unsigned pf = 0;
    {
      constexpr int QS = NH * DH * 2;
      const rsrc_t rwq = mk_rsrc(wq, DM * QS);
      const int grp = h >> 3;
      int ir = (lane >> 2) + b;
      ir = ir >= 9 ? ir - 9 : ir;
      const unsigned xown = ld4<NTL>(rx, ir * 128 + (lane & 3) * 4, 16 * a);
      const int vo = s * QS + e * 16, lo = 8 * a * QS + h * (DH * 2);
      int off = lo + b * (64 * QS);
      wb[0] = ld16<0>(rwq, vo, off);
      wb[1] = ld16<0>(rwq, vo + 128, off);
      CYC(off, 64 * QS, lo, DM * QS);
      v4u x0, x1, g0, g1;
      v2u eo;
      if (a == NA - 1) {
        const rsrc_t rg = mk_rsrc(gw, DM * 2);
        x0 = ld16<NTL>(rx, lane * 16, 0); x1 = ld16<NTL>(rx, lane * 16 + 1024, 0);
        g0 = ld16<NTL>(rg, lane * 16, 0); g1 = ld16<NTL>(rg, lane * 16 + 1024, 0);
        eo = __builtin_amdgcn_raw_buffer_load_b64(rws, (lane >> 6) * 8, b * 128, SC1);
      }
      if (ht < 8) cnt[ht] = 0u;
      SCHED_FENCE();
      asm volatile("s_waitcnt lgkmcnt(0)" ::: "memory");
      __builtin_amdgcn_s_barrier();
      SCHED_FENCE();
      tstart = now();
      const GAS unsigned* wp;
      {
        int hx = ht;
        PIN(hx);
        const int m = (h >> 3) * 8 + b, r9 = hx / 9;
        const char* p1 = (const char*)w1 + (size_t)(18 * m + r9) * (DF * 2) + (h & 7) * 1152 + (hx - 9 * r9) * 128;
        const char* p2 = (const char*)w2 + (size_t)(h & 7) * (576 * DM * 2) + (162 * m + hx - 162) * 128;
        const char* p3 = (const char*)wo + (size_t)h * (DH * DM * 2) + (144 * b + (hx < 468 ? hx : 467) - 324) * 128;
        wp = (const GAS unsigned*)(hx < 162 ? p1 : (hx < 324 ? p2 : p3));
      }
      if (grp != 0) {
        if (b == 7) {
          wb[2] = ld16<0>(rwq, vo, off); wb[3] = ld16<0>(rwq, vo + 128, off);
          asm volatile("" :: "v"(wb[2]), "v"(wb[3]));
        }
        SCHED_FENCE();
        if (grp >= 2) pf = *wp;
        SCHED_FENCE();
        sleep_until(tstart + T_Q1 + (grp - 1) * T_QD);
      }
      SCHED_FENCE();
#pragma unroll
      for (int i = 1; i < 9; ++i) {
        wb[2 * i] = ld16<0>(rwq, vo, off);
        wb[2 * i + 1] = ld16<0>(rwq, vo + 128, off);
        CYC(off, 64 * QS, lo, DM * QS);
      }
      SCHED_FENCE();
      {
        const unsigned hm = (e & 1) ? 0xffff0000u : 0x0000ffffu;
#pragma unroll
        for (int r = 0; r < 4; ++r) msk[r] = (e >> 1) == r ? hm : 0u;
      }
      const unsigned rsel = (s & 1) ? 0x03020302u : 0x01000100u;
#pragma unroll
      for (int i = 0; i < 9; ++i) {
        const unsigned pr = (unsigned)__builtin_amdgcn_ds_bpermute((4 * i + (s >> 1)) * 4, (int)xown);
        xs[i] = __builtin_amdgcn_perm(pr, pr, rsel);
        af[i] = AFRAG(xs[i]);
        PIN(af[i]);
      }
      SCHED_FENCE();
      v4f q0 = {0.f, 0.f, 0.f, 0.f}, q1 = {0.f, 0.f, 0.f, 0.f};
#pragma unroll
      for (int i = 0; i < 9; ++i) { q0 = mfma(af[i], wb[2 * i], q0); q1 = mfma(af[i], wb[2 * i + 1], q1); SCHED_FENCE(); }
      if (valid) {
        *(v4f*)(lds + L_Q + part * 128 + dcol) = q0;
        *(v4f*)(lds + L_Q + part * 128 + 64 + dcol) = q1;
      }
      lds_arrive(cnt + C_QPART, lane);
      if (grp < 2) { SCHED_FENCE(); pf = *wp; SCHED_FENCE(); }
      if (a == 0) {
        lds_wait<0>(cnt + C_QPART, NA);
        v2f t = *(const v2f*)(lds + L_Q + 2 * lane);
#pragma unroll
        for (int p = 1; p < 16; ++p) t += *(const v2f*)(lds + L_Q + p * 128 + 2 * lane);
        *(unsigned*)(lds + L_QP + lane) = pack_bf16(t[0], t[1]);
        lds_arrive(cnt + C_Q, lane);
      }
      __builtin_amdgcn_s_setprio(0);
      if (a == NA - 1) {
        PIN(eo); PIN(x0); PIN(x1); PIN(g0); PIN(g1);
        float s1 = 0.f, gd = 0.f;
#pragma unroll
        for (int i = 0; i < 4; ++i) {
          const unsigned u0 = x0[i], u1 = x1[i], w0 = g0[i], w1_ = g1[i];
          s1 += (lo16(u0) + hi16(u0)) + (lo16(u1) + hi16(u1));
          gd = dot2(u1, w1_, dot2(u0, w0, gd));
        }
        s1 = wave_sum(s1);
        gd = wave_sum(gd);
        const float mean = s1 * (1.0f / DM);
        float s2 = 0.f, s3 = 0.f;
#pragma unroll
        for (int i = 0; i < 4; ++i) {
          const unsigned u0 = x0[i], u1 = x1[i];
          const float v0 = lo16(u0) - mean, v1 = hi16(u0) - mean, v2 = lo16(u1) - mean, v3 = hi16(u1) - mean;
          s2 = fmaf(v0, v0, fmaf(v1, v1, s2));
          s3 = fmaf(v2, v2, fmaf(v3, v3, s3));
        }
        s2 += lane < 8 ? s3 : 0.f;
        s2 = wave_sum(s2);
        if (lane == 0) {
          *(v4f*)(lds + L_STAT) = v4f{mean, 1.0f / sqrtf(s2 * (1.0f / DM) + 1e-5f), 1.0f / (1.0f + __expf(-gd)), 0.f};
          *(u64*)(lds + L_TAG) = next_epoch(__builtin_bit_cast(u64, eo));
        }
        lds_arrive(cnt + C_STAT, lane);
      }
      SCHED_FENCE();
    }
    float gate;
    unsigned tlo, thi;
    {
      lds_wait<2>(cnt + C_STAT, 1);
      const v4f st = *(const v4f*)(lds + L_STAT);
      const u64 z = *(const u64*)(lds + L_TAG);
      gate = st[2];
#pragma unroll
      for (int i = 0; i < 9; ++i) {
        const float y = (lo16(xs[i]) - st[0]) * st[1];
        const unsigned rep = pack_bf16(y, y);
        af[i] = AFRAG(rep);
        PIN(af[i]);
      }
      tlo = (unsigned)z & 0xff000000u;
      thi = (unsigned)(z >> 32);
    }
    sleep_until(tstart + T_MLP);
    asm volatile("" :: "v"(pf));
    const int nu = (h >> 3) == 3 ? 3 : 2;
    const int f0 = (h & 7) * 576 + (h >> 3) * 128;
    __builtin_amdgcn_s_setprio(3);
    {
      const rsrc_t r1 = mk_rsrc(w1, DM * DF * 2);
      const int vo = s * (DF * 2) + e * 16;
#pragma unroll 1
      for (int u = 0; u < nu; ++u) {
        const int lo = 8 * a * (DF * 2) + f0 * 2 + u * 128;
        int off = lo + b * (64 * DF * 2);
        const unsigned eta = (unsigned)(now() - tstart) + (2 * nu + 2 - u) * (9 * T_WLOAD);
#pragma unroll
        for (int i = 0; i < 9; ++i) { one_or_two(eta); wb[i] = ld16<0>(r1, vo, off); CYC(off, 64 * DF * 2, lo, DM * DF * 2); }
        SCHED_FENCE();
        v4f d = {0.f, 0.f, 0.f, 0.f};
#pragma unroll
        for (int i = 0; i < 9; ++i) { d = mfma(af[i], wb[i], d); SCHED_FENCE(); }
        if (valid) *(v4f*)(lds + L_HP + (u * 16 + part) * 64 + dcol) = d;
      }
    }
    lds_arrive(cnt + C_HID, lane);
    const int vo2 = s * (DM * 2) + e * 16;
    {
      v4f am[9];
#pragma unroll
      for (int t = 0; t < 9; ++t) am[t] = v4f{0.f, 0.f, 0.f, 0.f};
      const rsrc_t r2 = mk_rsrc(w2 + (size_t)f0 * DM, nu * 64 * DM * 2);
      const rsrc_t rb1 = mk_rsrc(b1 + f0, nu * 128);
#pragma unroll 1
      for (int u = 0; u < nu; ++u) {
        const unsigned eta = (unsigned)(now() - tstart) + (nu + 2 - u) * (9 * T_WLOAD);
        const unsigned bias = __builtin_amdgcn_raw_buffer_load_b16(rb1, s * 2, u * 128 + a * 16, NTL);
#pragma unroll
        for (int t = 0; t < 9; ++t) { if (t) one_or_two(eta); wb[t] = ld16<0>(r2, vo2, (u * 64 + 8 * a) * (DM * 2) + t * 128); }
        SCHED_FENCE();
        lds_wait<1>(cnt + C_HID, NA);
        const float* hp = lds + L_HP + (u * 16 + 2 * e) * 64 + 8 * a + s;
        float hv = sum8(hp[0] + hp[64]) + lo16(bias);
        const float uu = 0.7978845608028654f * (hv + 0.044715f * hv * hv * hv);
        hv = hv / (1.0f + __expf(-2.0f * uu));
        const unsigned rep = pack_bf16(hv, hv);
        const v4u ah = AFRAG(rep);
#pragma unroll
        for (int t = 0; t < 9; ++t) { am[t] = mfma(ah, wb[t], am[t]); SCHED_FENCE(); }
      }
      if (valid) {
#pragma unroll
        for (int t = 0; t < 9; ++t) *(v4f*)(lds + L_OUT + part * OSTR + dcol + t * 64) = am[t];
      }
    }
    lds_arrive(cnt + C_MLP, lane);
    const unsigned bb = ld4<NTL>(mk_rsrc(b2, h == 0 ? DM * 2 : 0), ht * 4, 0);
    {
      const rsrc_t r3 = mk_rsrc(wo + (size_t)h * DH * DM, DH * DM * 2);
      const unsigned eta = (unsigned)(now() - tstart) + 18 * T_WLOAD;
      const int vo3 = g * (DM * 2) + c * 16;
#pragma unroll
      for (int q = 0; q < 4; ++q)
#pragma unroll
        for (int t = 0; t < 4; ++t) { one_or_two(eta); wb[4 * q + t] = ld16<0>(r3, vo3, (16 * a + 4 * q) * (DM * 2) + t * 256); }
#pragma unroll
      for (int k = 0; k < 2; ++k) { one_or_two(eta); wb[16 + k] = ld16<0>(r3, vo2, (16 * a + 8 * k) * (DM * 2) + 1024); }
    }
    SCHED_FENCE();
    __builtin_amdgcn_s_setprio(0);
    const int tq = ht < 288 ? ht : 287;
    v2f mlp = v2f{lo16(bb), hi16(bb)};
    lds_wait<1>(cnt + C_MLP, NA);
#pragma unroll
    for (int p = 0; p < 16; ++p) mlp += *(const v2f*)(lds + L_OUT + p * OSTR + 2 * tq);
    mlp *= 1.0f - gate;
    PIN(mlp);
#pragma unroll
    for (int i = 0; i < 18; ++i) PIN(wb[i]);
    float fac;
    {
      lds_wait<16>(cnt + C_ML, NS);
      const v4f m01 = *(const v4f*)(lds + L_ML), m23 = *(const v4f*)(lds + L_ML + 4);
      const float M = fmaxf(fmaxf(m01[0], m01[2]), fmaxf(m23[0], m23[2]));
      float L = __builtin_amdgcn_exp2f(m01[0] - M) * m01[1];
      L = fmaf(__builtin_amdgcn_exp2f(m01[2] - M), m01[3], L);
      L = fmaf(__builtin_amdgcn_exp2f(m23[0] - M), m23[1], L);
      L = fmaf(__builtin_amdgcn_exp2f(m23[2] - M), m23[3], L);
      fac = __builtin_amdgcn_exp2f(lds[L_ML + (e >> 1) * 2] - M) / L;
      PIN(fac);
    }
    int gvo = (ht >> 4) * (NH * 256) + (ht & 15) * 16;
    unsigned mlo[4], mhi[4];
#pragma unroll
    for (int r = 0; r < 4; ++r) { mlo[r] = c < 8 ? msk[r] : 0u; mhi[r] = c < 8 ? 0u : msk[r]; PIN(mlo[r]); PIN(mhi[r]); }
    LAS float* l3 = (LAS float*)lds;
    LAS float* pp = l3 + L_PART + e * 128 + 16 * a + 8 * (c >> 3) + g;
    LAS v4f* ow = (LAS v4f*)(l3 + L_OUT + a * OSTR + 128 * (g >> 1) + 8 * c + 4 * (g & 1));
    LAS v4f* o8 = (LAS v4f*)(l3 + (valid ? L_OUT + part * OSTR + dcol + 512 : L_HP + lane * 4));
    LAS v2f* rd = (LAS v2f*)(l3 + L_OUT + 2 * tq);
    asm volatile("" : "+v"(gvo), "+v"(pp), "+v"(ow), "+v"(o8), "+v"(rd), "+v"(tlo), "+v"(thi));
    __builtin_amdgcn_s_setprio(3);
    SCHED_FENCE();
    __syncthreads();
    {
      float t0 = pp[0] * fac, t1 = pp[4] * fac;
      PIN(t0); PIN(t1);
      const unsigned own = pack_bf16(sum8(t0), sum8(t1));
      const unsigned oth = dppu<XOR8>(own);
      const unsigned p01 = c < 8 ? own : oth, p23 = c < 8 ? oth : own;
      const unsigned rq[4] = {__builtin_amdgcn_perm(p01, p01, 0x01000100u), __builtin_amdgcn_perm(p01, p01, 0x03020302u),
                              __builtin_amdgcn_perm(p23, p23, 0x01000100u), __builtin_amdgcn_perm(p23, p23, 0x03020302u)};
      v4u fl[4], fh[4];
#pragma unroll
      for (int q = 0; q < 4; ++q) {
        fl[q] = v4u{rq[q] & mlo[0], rq[q] & mlo[1], rq[q] & mlo[2], rq[q] & mlo[3]};
        fh[q] = v4u{rq[q] & mhi[0], rq[q] & mhi[1], rq[q] & mhi[2], rq[q] & mhi[3]};
      }
      v4f d0 = {0.f, 0.f, 0.f, 0.f}, d1 = {0.f, 0.f, 0.f, 0.f};
#pragma unroll
      for (int q = 0; q < 4; ++q) { d0 = mfma(fl[q], wb[4 * q], d0); d0 = mfma(fh[q], wb[4 * q + 1], d0); }
      const v4f d8 = mfma(fl[2] | fh[3], wb[17], mfma(fl[0] | fh[1], wb[16], v4f{0.f, 0.f, 0.f, 0.f}));
      ow[0] = d0;
#pragma unroll
      for (int q = 0; q < 4; ++q) { d1 = mfma(fl[q], wb[4 * q + 2], d1); d1 = mfma(fh[q], wb[4 * q + 3], d1); }
      *o8 = d8;
      ow[64] = d1;
    }
    __syncthreads();
    if (a < 5) {
      constexpr int RS = OSTR / 2;
      v2f t = ((rd[0] + rd[RS]) + (rd[2 * RS] + rd[3 * RS])) + ((rd[4 * RS] + rd[5 * RS]) + (rd[6 * RS] + rd[7 * RS]));
      if (a == 4) t += ((rd[8 * RS] + rd[9 * RS]) + (rd[10 * RS] + rd[11 * RS])) + ((rd[12 * RS] + rd[13 * RS]) + (rd[14 * RS] + rd[15 * RS]));
      const v4u gr = make_granules(fmaf(gate, t[0], mlp[0]), fmaf(gate, t[1], mlp[1]), tlo, thi);
      if (ht < 288) __builtin_amdgcn_raw_buffer_store_b128(gr, rws, gvo, WS_GO + b * (NCONS * NH * 256) + h * 256, SC1);
    }
  } else {
    int lane = tid & 63;
    asm volatile("; streaming" : "+v"(lane) : "s"(kc) : "memory");
    __builtin_amdgcn_s_setprio(2);
    const int sw = wave;
    const int c = lane & 15;
    constexpr int HB = SK * DH * 2;
    const rsrc_t rk = mk_rsrc(kc + (size_t)bid * (HB / 2), HB);
    const rsrc_t rv = mk_rsrc(vc + (size_t)bid * (HB / 2), HB);
    const int vo = lane * 16 + sw * 1024;
    constexpr int LS = NS * 1024;
    v4u buf[W], pre[W];
    SCHED_FENCE();
#pragma unroll
    for (int j = 0; j < 4; ++j) pre[j] = ld16<NTL>(rk, vo, j * LS);
    SCHED_FENCE();
    __builtin_amdgcn_s_barrier();
    SCHED_FENCE();
    const unsigned t0 = (unsigned)now() + T_OFF;
    SCHED_FENCE();
#pragma unroll
    for (int j = 4; j < W; ++j) { pre[j] = ld16<NTL>(rk, vo, j * LS); SCHED_FENCE(); }
#pragma unroll
    for (int j = 0; j < W; ++j) { buf[j] = ld16<NTL>(rk, vo, W * LS + j * LS); SCHED_FENCE(); }
    lds_wait<0>(cnt + C_Q, 1);
    const v4u qp = *(const v4u*)(lds + L_QP + 4 * c);
    constexpr int NBLK = SK / NS / 4 / W;
    float s[NBLK];
#pragma unroll
    for (int i = 0; i < NBLK; ++i) s[i] = 0.f;
#define TR_STEP(xs, HALF, CTRL)                                     \
  _Pragma("unroll") for (int u = 0; u < (HALF); ++u) {              \
    const float lo_ = xs[u], hi_ = xs[u + (HALF)];                  \
    const float t1_ = lo_ + dpp<CTRL>(lo_), t2_ = hi_ + dpp<CTRL>(hi_); \
    xs[u] = (c & (HALF)) ? t2_ : t1_;                               \
  }
#define TR(xs) TR_STEP(xs, 8, XOR8) TR_STEP(xs, 4, XOR7) TR_STEP(xs, 2, XOR2) TR_STEP(xs, 1, XOR1)
#define K_DOT(cur, dst)                                             \
  {                                                                 \
    float d = dot2(cur[0], qp[0], 0.f);                             \
    d = dot2(cur[1], qp[1], d);                                     \
    d = dot2(cur[2], qp[2], d);                                     \
    d = dot2(cur[3], qp[3], d);                                     \
    PIN(d);                                                         \
    dst = d;                                                        \
  }
    {
      float xs[W], x0[W];
      int lim = 0;
#pragma unroll
      for (int j = 0; j < W; ++j) {
        if ((j & 7) == 0) lim = pick_lim(t0, 2 * W + j, sw + (j >> 3));
        const v4u cur = buf[j];
        K_DOT(cur, xs[j])
        wait_lim(lim);
        buf[j] = ld16<NTL>(rk, vo, 2 * W * LS + j * LS);
        SCHED_FENCE();
        const v4u old = pre[j];
        K_DOT(old, x0[j])
        SCHED_FENCE();
      }
      TR(xs)
      TR(x0)
      s[0] = x0[0];
      s[1] = xs[0];
    }
#pragma unroll 1
    for (int blk = 2; blk < NBLK; ++blk) {
      const bool last = blk + 1 == NBLK;
      const rsrc_t rn = last ? rv : rk;
      const int so = last ? 0 : (blk + 1) * (W * LS);
      float xs[W];
      int lim = 0;
#pragma unroll
      for (int j = 0; j < W; ++j) {
        if ((j & 7) == 0) lim = pick_lim(t0, (blk + 1) * W + j, sw + 2 * blk + (j >> 3));
        const v4u cur = buf[j];
        K_DOT(cur, xs[j])
        wait_lim(lim);
        buf[j] = ld16<NTL>(rn, vo, so + j * LS);
        SCHED_FENCE();
      }
      TR(xs)
#pragma unroll
      for (int i = 2; i + 1 < NBLK; ++i) s[i] = s[i + 1];
      s[NBLK - 1] = xs[0];
    }
#undef TR
#undef TR_STEP
#undef K_DOT
    constexpr float C2 = 0.088388347648318440f * 1.4426950408889634f;
    unsigned pp[NBLK];
    {
      float m = s[0];
#pragma unroll
      for (int i = 1; i < NBLK; ++i) m = fmaxf(m, s[i]);
      m = wave_max(m);
      float l = 0.f;
#pragma unroll
      for (int i = 0; i < NBLK; ++i) {
        const float p = __builtin_amdgcn_exp2f((s[i] - m) * C2);
        l += p;
        const float ph = hi16(__builtin_bit_cast(unsigned, p));
        pp[i] = pack_bf16(ph, p - ph);
      }
      l = wave_sum(l);
      if (lane == 0) *(v2f*)(lds + L_ML + 2 * sw) = v2f{m * C2, l};
      lds_arrive(cnt + C_ML, lane);
    }
    unsigned sel[4];
    {
      const unsigned src = (c & 8) ? 0x0302u : 0x0100u;
      const unsigned pl = (c & 1) ? (src << 16) | 0x0c0cu : 0x0c0c0000u | src;
#pragma unroll
      for (int r = 0; r < 4; ++r) sel[r] = ((c & 7) >> 1) == r ? pl : 0x0c0c0c0cu;
    }
    v4f acc = {0.f, 0.f, 0.f, 0.f};
#define V_STEP(J, REFILL)                                           \
  {                                                                 \
    const unsigned pb = dppu<BCAST + (J)>(p);                       \
    v4u af;                                                         \
    _Pragma("unroll") for (int r = 0; r < 4; ++r) af[r] = __builtin_amdgcn_perm(pb, pb, sel[r]); \
    acc = mfma(af, buf[J], acc);                                    \
    PIN(acc);                                                       \
    if (REFILL) {                                                   \
      if (((J) & 7) == 0) lim = pick_lim(t0, (NBLK + blk + 1) * W + (J), sw + 2 * blk + ((J) >> 3)); \
      wait_lim(lim);                                                \
      buf[J] = ld16<NTL>(rv, vo, so + (J) * LS);                    \
    }                                                               \
    SCHED_FENCE();                                                  \
  }
#define V_BLOCK(REFILL)                                             \
  V_STEP(0, REFILL) V_STEP(1, REFILL) V_STEP(2, REFILL) V_STEP(3, REFILL)     \
  V_STEP(4, REFILL) V_STEP(5, REFILL) V_STEP(6, REFILL) V_STEP(7, REFILL)     \
  V_STEP(8, REFILL) V_STEP(9, REFILL) V_STEP(10, REFILL) V_STEP(11, REFILL)   \
  V_STEP(12, REFILL) V_STEP(13, REFILL) V_STEP(14, REFILL) V_STEP(15, REFILL)
#pragma unroll 1
    for (int blk = 0; blk + 1 < NBLK; ++blk) {
      const int so = (blk + 1) * (W * LS);
      const unsigned p = pp[0];
      int lim = 0;
      V_BLOCK(true)
#pragma unroll
      for (int i = 0; i + 1 < NBLK; ++i) pp[i] = pp[i + 1];
    }
    {
      const int so = 0, blk = 0;
      const unsigned p = pp[0];
      int lim = 0;
      V_BLOCK(false)
    }
#undef V_BLOCK
#undef V_STEP
    *(v4f*)(lds + L_PART + sw * 256 + (lane >> 5) * 128 + c * 8 + ((lane >> 4) & 1) * 4) = acc;
    __syncthreads();
    const rsrc_t rws = mk_rsrc(ws, WS_END);
    const u64 z = *(const u64*)(lds + L_TAG);
    int go = (lane >> 2) * 256 + (lane & 3) * 16;
    GAS unsigned* op = (GAS unsigned*)(out + b * DM + 32 * h + 8 * sw) + (lane & 3);
    asm volatile("" : "+v"(go), "+v"(op));
    __builtin_amdgcn_s_barrier();
    if (h < NCONS) {
      const unsigned tlo = (unsigned)z & 0xff000000u, thi = (unsigned)(z >> 32);
      const int so = WS_GO + (b * NCONS + h) * (NH * 256) + sw * 64;
      sleep_until(now() + T_POLL);
      v4u v0, v1;
      for (unsigned spins = 0;; ++spins) {
        v0 = ld16<SC1>(rws, go, so);
        v1 = ld16<SC1>(rws, go + 4096, so);
        if (__all(granules_ok(v0, tlo, thi) & granules_ok(v1, tlo, thi))) break;
        if (spins > SPIN_CAP) { v0[0] = v0[2] = 0x7fc000u; break; }
        asm volatile("" ::: "memory");
      }
      const unsigned a0 = v0[0], a1 = v0[2], b0 = v1[0], b1_ = v1[2];
      float r0 = granule_val(a0) + granule_val(b0), r1 = granule_val(a1) + granule_val(b1_);
      r0 += dpp<ROR4>(r0); r1 += dpp<ROR4>(r1);
      r0 += dpp<XOR8>(r0); r1 += dpp<XOR8>(r1);
      r0 = sum_x32(sum_x16(r0));
      r1 = sum_x32(sum_x16(r1));
      if (lane < 4) *op = pack_bf16(r0, r1);
      if (h == 0 && sw == 0 && lane == 0) __hip_atomic_store((GAS u64*)(ws + b * 128), z, __ATOMIC_RELAXED, __HIP_MEMORY_SCOPE_AGENT);
    }
  }
}

}

extern "C" void kernel_launch(void* const* d_in, const int* in_sizes, int n_in, void* d_out, int out_size,
                              void* d_ws, size_t ws_size, hipStream_t stream) {
  using namespace gm;
  const u16* x = (const u16*)d_in[0];
  const u16* kc = (const u16*)d_in[1];
  const u16* vc = (const u16*)d_in[2];
  const u16* wq = (const u16*)d_in[3];
  const u16* wo = (const u16*)d_in[4];
  const u16* w1 = (const u16*)d_in[6];
  const u16* b1 = (const u16*)d_in[7];
  const u16* w2 = (const u16*)d_in[8];
  const u16* b2 = (const u16*)d_in[9];
  const u16* gw = (const u16*)d_in[10];
  hipLaunchKernelGGL(fused_main, dim3(NB * NH), dim3(NT), 0, stream, x, kc, vc, wq, wo, w1, b1, w2, b2, gw, (unsigned char*)d_ws, (u16*)d_out);
}
```

```cpp
#include <hip/hip_runtime.h>
#include <hip/hip_bf16.h>
#include <stdint.h>

namespace gm {

typedef unsigned v2u __attribute__((ext_vector_type(2)));
typedef unsigned v4u __attribute__((ext_vector_type(4)));
typedef float v2f __attribute__((ext_vector_type(2)));
typedef float v4f __attribute__((ext_vector_type(4)));
typedef __bf16 bf16x2 __attribute__((ext_vector_type(2)));
typedef __bf16 bf16x8 __attribute__((ext_vector_type(8)));
typedef unsigned short u16;
typedef unsigned long long u64;
typedef __amdgpu_buffer_rsrc_t rsrc_t;
#define GAS __attribute__((address_space(1)))
#define LAS __attribute__((address_space(3)))
#define CAS __attribute__((address_space(4)))

constexpr int NB = 8, NH = 32, SK = 4096, DH = 128, DM = 576, DF = 4608;
constexpr int NS = 4;
constexpr int NA = 8;
constexpr int NT = (NS + NA) * 64;
constexpr int W = 16;
constexpr int NTL = 2, SC1 = 16;
constexpr unsigned SPIN_CAP = 1u << 18;
constexpr int OOB = 0x40000000;

constexpr int T_STREAM = 6910, T_OFF = 310;
constexpr int FLY_MID = 23, FLY_GAIN = 8;
constexpr int FLY_N2 = 352, FLY_MID2 = 21, FLY_GAIN2 = 16, FLY_LO = 18, FLY_HI = 32;
constexpr int NCP = 3, CP_STEP = 128, T_CP = 250;
constexpr int T_Q1 = 220, T_QD = 115;
constexpr int T_MLP = 900;
constexpr int T_WLOAD = 80, T_WEND = 7000;
constexpr int T_POLL = 60;

#define PIN(v) asm volatile("" : "+v"(v))
#define SCHED_FENCE() __builtin_amdgcn_sched_barrier(0)
#define RAW_BARRIER() do { asm volatile("" ::: "memory"); __builtin_amdgcn_s_barrier(); asm volatile("" ::: "memory"); } while (0)

__device__ __forceinline__ rsrc_t mk_rsrc(const void* p, unsigned bytes) {
  return __builtin_amdgcn_make_buffer_rsrc(const_cast<void*>(p), 0, bytes, 0x00020000);
}
template <int AUX> __device__ __forceinline__ v4u ld16(rsrc_t r, int voff, int soff) {
  return __builtin_amdgcn_raw_buffer_load_b128(r, voff, soff, AUX);
}
template <int AUX> __device__ __forceinline__ unsigned ld4(rsrc_t r, int voff, int soff) {
  return __builtin_amdgcn_raw_buffer_load_b32(r, voff, soff, AUX);
}
__device__ __forceinline__ float lo16(unsigned u) { return __builtin_bit_cast(float, u << 16); }
__device__ __forceinline__ float hi16(unsigned u) { return __builtin_bit_cast(float, u & 0xffff0000u); }
__device__ __forceinline__ float dot2(unsigned a, unsigned b, float acc) {
  return __builtin_amdgcn_fdot2_f32_bf16(__builtin_bit_cast(bf16x2, a), __builtin_bit_cast(bf16x2, b), acc, false);
}
__device__ __forceinline__ unsigned pack_bf16(float lo, float hi) {
  bf16x2 r;
  r[0] = (__bf16)lo;
  r[1] = (__bf16)hi;
  return __builtin_bit_cast(unsigned, r);
}
__device__ __forceinline__ v4f mfma(v4u a, v4u b, v4f c) {
  return __builtin_amdgcn_mfma_f32_16x16x32_bf16(__builtin_bit_cast(bf16x8, a), __builtin_bit_cast(bf16x8, b), c, 0, 0, 0);
}
template <int CTRL> __device__ __forceinline__ unsigned dppu(unsigned x) {
  return (unsigned)__builtin_amdgcn_mov_dpp((int)x, CTRL, 0xf, 0xf, true);
}
template <int CTRL> __device__ __forceinline__ float dpp(float x) {
  return __builtin_bit_cast(float, dppu<CTRL>(__builtin_bit_cast(unsigned, x)));
}
constexpr int XOR1 = 0xB1, XOR2 = 0x4E, XOR7 = 0x141, XOR8 = 0x128, ROR4 = 0x124, BCAST = 0x150;
__device__ __forceinline__ float sum_x16(float x) {
  auto s = __builtin_amdgcn_permlane16_swap(__float_as_uint(x), __float_as_uint(x), false, false);
  return __uint_as_float(s[0]) + __uint_as_float(s[1]);
}
__device__ __forceinline__ float sum_x32(float x) {
  auto t = __builtin_amdgcn_permlane32_swap(__float_as_uint(x), __float_as_uint(x), false, false);
  return __uint_as_float(t[0]) + __uint_as_float(t[1]);
}
__device__ __forceinline__ float sum8(float x) {
  x += dpp<XOR7>(x); x += dpp<XOR2>(x); x += dpp<XOR1>(x);
  return x;
}
__device__ __forceinline__ float wave_sum(float x) { x += dpp<XOR8>(x); return sum_x32(sum_x16(sum8(x))); }
__device__ __forceinline__ float wave_max(float x) {
  x = fmaxf(x, dpp<XOR8>(x)); x = fmaxf(x, dpp<XOR7>(x)); x = fmaxf(x, dpp<XOR2>(x)); x = fmaxf(x, dpp<XOR1>(x));
  auto s = __builtin_amdgcn_permlane16_swap(__float_as_uint(x), __float_as_uint(x), false, false);
  x = fmaxf(__uint_as_float(s[0]), __uint_as_float(s[1]));
  auto t = __builtin_amdgcn_permlane32_swap(__float_as_uint(x), __float_as_uint(x), false, false);
  return fmaxf(__uint_as_float(t[0]), __uint_as_float(t[1]));
}
__device__ __forceinline__ u64 now() { return __builtin_amdgcn_s_memrealtime(); }
__device__ __forceinline__ void sleep_until(u64 t) { while (now() < t) __builtin_amdgcn_s_sleep(2); }
__device__ __forceinline__ void lds_arrive(unsigned* cnt, int lane) {
  __builtin_amdgcn_fence(__ATOMIC_RELEASE, "workgroup");
  if (lane == 0) __hip_atomic_fetch_add(cnt, 1u, __ATOMIC_RELAXED, __HIP_MEMORY_SCOPE_WORKGROUP);
}
template <int SLP> __device__ __forceinline__ void lds_wait(unsigned* cnt, unsigned n) {
  while (__builtin_amdgcn_readfirstlane(__hip_atomic_load(cnt, __ATOMIC_RELAXED, __HIP_MEMORY_SCOPE_WORKGROUP)) < n)
    if (SLP) __builtin_amdgcn_s_sleep(SLP);
  __builtin_amdgcn_fence(__ATOMIC_ACQUIRE, "workgroup");
}
__device__ __forceinline__ u64 next_epoch(u64 z) {
  z += 0x9E3779B97F4A7C15ull;
  z = (z ^ (z >> 30)) * 0xBF58476D1CE4E5B9ull;
  z = (z ^ (z >> 27)) * 0x94D049BB133111EBull;
  return z ^ (z >> 31);
}
__device__ __forceinline__ v4u make_granules(float v0, float v1, unsigned tlo, unsigned thi) {
  return v4u{tlo | ((__builtin_bit_cast(unsigned, v0) + 0x80u) >> 8), thi, tlo | ((__builtin_bit_cast(unsigned, v1) + 0x80u) >> 8), thi};
}
__device__ __forceinline__ bool granules_ok(v4u v, unsigned tlo, unsigned thi) {
  return ((((v[0] ^ tlo) | (v[2] ^ tlo)) & 0xff000000u) | (v[1] ^ thi) | (v[3] ^ thi)) == 0u;
}
__device__ __forceinline__ float granule_val(unsigned lo) { return __builtin_bit_cast(float, lo << 8); }

__device__ __forceinline__ void wait_lim(int lim) {
  asm volatile(
      "s_waitcnt vmcnt(7)\n\t"
      "s_cmp_gt_i32 %0, 7\n\ts_cbranch_scc1 1f\n\ts_waitcnt vmcnt(6)\n\t"
      "s_cmp_gt_i32 %0, 6\n\ts_cbranch_scc1 1f\n\ts_waitcnt vmcnt(5)\n\t"
      "s_cmp_gt_i32 %0, 5\n\ts_cbranch_scc1 1f\n\ts_waitcnt vmcnt(4)\n\t"
      "s_cmp_gt_i32 %0, 4\n\ts_cbranch_scc1 1f\n\ts_waitcnt vmcnt(3)\n\t"
      "1:\n" :: "s"(lim) : "scc");
}
__device__ __forceinline__ int pick_lim(unsigned t0, int n0, int rot) {
  const int late = (int)((unsigned)now() - t0) - ((n0 * T_STREAM) >> 9);
  int tot = n0 >= FLY_N2 ? FLY_MID2 + ((late * FLY_GAIN2) >> 8) : FLY_MID + ((late * FLY_GAIN) >> 8);
  tot = tot < FLY_LO ? FLY_LO : (tot > FLY_HI ? FLY_HI : tot);
  return __builtin_amdgcn_readfirstlane((tot + (rot & 3)) >> 2);
}
__device__ __forceinline__ void one_or_two(unsigned eta) {
  SCHED_FENCE();
  asm volatile("s_waitcnt vmcnt(1)\n\ts_cmp_ge_u32 %0, %1\n\ts_cbranch_scc1 1f\n\ts_waitcnt vmcnt(0)\n\t1:\n" :: "s"(eta), "i"(T_WEND) : "scc");
  SCHED_FENCE();
}
#define CYC(off, step, lo, span) do { off += (step); off = off >= (lo) + (span) ? off - (span) : off; asm volatile("" : "+s"(off)); } while (0)

constexpr int page_j(int j) { return ((j & 1) << 13) | ((j & 2) << 14) | ((j & 4) << 10) | ((j & 8) << 13); }
__device__ __forceinline__ int page_blk(int blk) { return ((blk & 1) << 19) | ((blk & 2) << 17) | ((blk & 4) << 12) | ((blk & 8) << 14); }

constexpr int L_Q = 0;
constexpr int PSTR = DH + 4;
constexpr int L_PART = L_Q + 16 * 128;
constexpr int L_ML = L_PART + NS * 2 * PSTR;
constexpr int L_TAG = L_ML + 8;
constexpr int L_STAT = L_TAG + 4;
constexpr int L_CNT = L_STAT + 4;
constexpr int L_QP = L_CNT + 8;
constexpr int L_HP = L_QP + 64;
constexpr int L_OUT = L_HP + 3 * 16 * 64;
constexpr int OSTR = DM + 4;
constexpr int L_END = L_OUT + 16 * OSTR;
enum { C_HID = 0, C_MLP = 1, C_ML = 2, C_QPART = 3, C_Q = 4, C_STAT = 5, C_SCHED = 6   };

constexpr int NCONS = DM / 32;
constexpr int WS_CLK = 1024, WS_GO = 4096, WS_END = WS_GO + NB * NCONS * NH * 256;

__global__ void __launch_bounds__(NT, 3)
fused_main(const u16* a_kc, unsigned char* a_ws, const u16* a_x, const u16* a_gw, const u16* a_wq, const u16* a_wo,
           const u16* a_w1, const u16* a_w2, const u16* a_vc, u16* a_out, const u16* a_b1, const u16* a_b2) {
  __shared__ __attribute__((aligned(16))) float lds[L_END];
  typedef u64 v8q __attribute__((ext_vector_type(8)));
  typedef u64 v2q __attribute__((ext_vector_type(2)));
  const CAS u64* ka = (const CAS u64*)__builtin_amdgcn_kernarg_segment_ptr();
  asm volatile("" : "+s"(ka));
  const v8q kv = *(const CAS v8q*)ka;
  const int bid = blockIdx.x, b = bid >> 5, h = bid & 31;
  const int tid = threadIdx.x;
  const int wave = __builtin_amdgcn_readfirstlane(tid >> 6);
  unsigned* cnt = (unsigned*)(lds + L_CNT);

  if (wave >= NS) {
    int lane = tid & 63;
    if (tid < NS * 64 + 8) cnt[tid - NS * 64] = 0u;
    asm volatile("s_waitcnt lgkmcnt(0) ; helper" : "+v"(lane) :: "memory");
    unsigned char* ws = (unsigned char*)kv[1];
    const u16 *x = (const u16*)kv[2], *gw = (const u16*)kv[3], *wq = (const u16*)kv[4], *wo = (const u16*)kv[5], *w1 = (const u16*)kv[6], *w2 = (const u16*)kv[7];
    const rsrc_t rws = mk_rsrc(ws, WS_END);
    __builtin_amdgcn_s_setprio(3);
    const int g = lane >> 4, c = lane & 15;
    const int a = wave - NS;
    const int ht = tid - NS * 64;
    const int s = g + 4 * (c >> 3), e = c & 7;
    unsigned msk[4];
#define AFRAG(rep) v4u{(rep) & msk[0], (rep) & msk[1], (rep) & msk[2], (rep) & msk[3]}
    const bool valid = (g >> 1) == (c >> 3);
    const int dcol = 8 * e + 4 * (g & 1);
    const int part = 2 * a + (c >> 3);
    u64 tstart;
    const rsrc_t rx = mk_rsrc(x + b * DM, DM * 2);
    v4u af[9];
    v4u wb[18];
    unsigned xs[9];
    unsigned pf = 0;
    {
      constexpr int QS = NH * DH * 2;
      const rsrc_t rwq = mk_rsrc(wq, DM * QS);
      const int grp = h >> 3;
      int ir = (lane >> 2) + b;
      ir = ir >= 9 ? ir - 9 : ir;
      const unsigned xown = ld4<0>(rx, ir * 128 + (lane & 3) * 4, 16 * a);
      const int vo = s * QS + e * 16, lo = 8 * a * QS + h * (DH * 2);
      int off = lo + b * (64 * QS);
      wb[0] = ld16<0>(rwq, vo, off);
      wb[1] = ld16<0>(rwq, vo + 128, off);
      CYC(off, 64 * QS, lo, DM * QS);
      v4u x0, x1, g0, g1;
      v2u eo;
      if (a == NA - 1) {
        const rsrc_t rg = mk_rsrc(gw, DM * 2);
        x0 = ld16<0>(rx, lane * 16, 0); x1 = ld16<0>(rx, lane * 16 + 1024, 0);
        g0 = ld16<0>(rg, lane * 16, 0); g1 = ld16<0>(rg, lane * 16 + 1024, 0);
        eo = __builtin_amdgcn_raw_buffer_load_b64(rws, (lane >> 6) * 8, b * 128, SC1);
      }
      SCHED_FENCE();
      RAW_BARRIER();
      SCHED_FENCE();
      tstart = now();
      if (bid < 8 && a == NA - 2) __builtin_amdgcn_raw_buffer_store_b32((unsigned)tstart, rws, lane == 0 ? 0 : OOB, WS_CLK + bid * 4, SC1);
      const GAS unsigned* wp;
      {
        int hx = ht;
        PIN(hx);
        const int m = (h >> 3) * 8 + b, r9 = hx / 9;
        const char* p1 = (const char*)w1 + (size_t)(18 * m + r9) * (DF * 2) + (h & 7) * 1152 + (hx - 9 * r9) * 128;
        const char* p2 = (const char*)w2 + (size_t)(h & 7) * (576 * DM * 2) + (162 * m + hx - 162) * 128;
        const char* p3 = (const char*)wo + (size_t)h * (DH * DM * 2) + (144 * b + (hx < 468 ? hx : 467) - 324) * 128;
        wp = (const GAS unsigned*)(hx < 162 ? p1 : (hx < 324 ? p2 : p3));
      }
      if (grp != 0) {
        if (b == 7) {
          wb[2] = ld16<0>(rwq, vo, off); wb[3] = ld16<0>(rwq, vo + 128, off);
          asm volatile("" :: "v"(wb[2]), "v"(wb[3]));
        }
        SCHED_FENCE();
        if (grp >= 2) pf = *wp;
        SCHED_FENCE();
        sleep_until(tstart + T_Q1 + (grp - 1) * T_QD);
      }
      SCHED_FENCE();
#pragma unroll
      for (int i = 1; i < 9; ++i) {
        wb[2 * i] = ld16<0>(rwq, vo, off);
        wb[2 * i + 1] = ld16<0>(rwq, vo + 128, off);
        CYC(off, 64 * QS, lo, DM * QS);
      }
      SCHED_FENCE();
      {
        const unsigned hm = (e & 1) ? 0xffff0000u : 0x0000ffffu;
#pragma unroll
        for (int r = 0; r < 4; ++r) msk[r] = (e >> 1) == r ? hm : 0u;
      }
      const unsigned rsel = (s & 1) ? 0x03020302u : 0x01000100u;
#pragma unroll
      for (int i = 0; i < 9; ++i) {
        const unsigned pr = (unsigned)__builtin_amdgcn_ds_bpermute((4 * i + (s >> 1)) * 4, (int)xown);
        xs[i] = __builtin_amdgcn_perm(pr, pr, rsel);
        af[i] = AFRAG(xs[i]);
        PIN(af[i]);
      }
      SCHED_FENCE();
      v4f q0 = {0.f, 0.f, 0.f, 0.f}, q1 = {0.f, 0.f, 0.f, 0.f};
#pragma unroll
      for (int i = 0; i < 9; ++i) { q0 = mfma(af[i], wb[2 * i], q0); q1 = mfma(af[i], wb[2 * i + 1], q1); SCHED_FENCE(); }
      if (valid) {
        *(v4f*)(lds + L_Q + part * 128 + dcol) = q0;
        *(v4f*)(lds + L_Q + part * 128 + 64 + dcol) = q1;
      }
      lds_arrive(cnt + C_QPART, lane);
      if (grp < 2) { SCHED_FENCE(); pf = *wp; SCHED_FENCE(); }
      if (a == 0) {
        lds_wait<0>(cnt + C_QPART, NA);
        v2f t = *(const v2f*)(lds + L_Q + 2 * lane);
#pragma unroll
        for (int p = 1; p < 16; ++p) t += *(const v2f*)(lds + L_Q + p * 128 + 2 * lane);
        *(unsigned*)(lds + L_QP + lane) = pack_bf16(t[0], t[1]);
        lds_arrive(cnt + C_Q, lane);
      }
      __builtin_amdgcn_s_setprio(0);
      if (a == NA - 1) {
        PIN(eo); PIN(x0); PIN(x1); PIN(g0); PIN(g1);
        float s1 = 0.f, gd = 0.f;
#pragma unroll
        for (int i = 0; i < 4; ++i) {
          const unsigned u0 = x0[i], u1 = x1[i], w0 = g0[i], w1_ = g1[i];
          s1 += (lo16(u0) + hi16(u0)) + (lo16(u1) + hi16(u1));
          gd = dot2(u1, w1_, dot2(u0, w0, gd));
        }
        s1 = wave_sum(s1);
        gd = wave_sum(gd);
        const float mean = s1 * (1.0f / DM);
        float s2 = 0.f, s3 = 0.f;
#pragma unroll
        for (int i = 0; i < 4; ++i) {
          const unsigned u0 = x0[i], u1 = x1[i];
          const float v0 = lo16(u0) - mean, v1 = hi16(u0) - mean, v2 = lo16(u1) - mean, v3 = hi16(u1) - mean;
          s2 = fmaf(v0, v0, fmaf(v1, v1, s2));
          s3 = fmaf(v2, v2, fmaf(v3, v3, s3));
        }
        s2 += lane < 8 ? s3 : 0.f;
        s2 = wave_sum(s2);
        if (lane == 0) {
          *(v4f*)(lds + L_STAT) = v4f{mean, 1.0f / sqrtf(s2 * (1.0f / DM) + 1e-5f), 1.0f / (1.0f + __expf(-gd)), 0.f};
          *(u64*)(lds + L_TAG) = next_epoch(__builtin_bit_cast(u64, eo));
        }
        lds_arrive(cnt + C_STAT, lane);
      }
      SCHED_FENCE();
    }
    float gate;
    unsigned tlo, thi;
    {
      lds_wait<2>(cnt + C_STAT, 1);
      const v4f st = *(const v4f*)(lds + L_STAT);
      const u64 z = *(const u64*)(lds + L_TAG);
      gate = st[2];
#pragma unroll
      for (int i = 0; i < 9; ++i) {
        const float y = (lo16(xs[i]) - st[0]) * st[1];
        const unsigned rep = pack_bf16(y, y);
        af[i] = AFRAG(rep);
        PIN(af[i]);
      }
      tlo = (unsigned)z & 0xff000000u;
      thi = (unsigned)(z >> 32);
    }
    sleep_until(tstart + T_MLP);
    int knext = 0, shift = 0;
    auto monitor = [&]() {
      const int el = (int)(now() - tstart) - shift - T_OFF - T_CP;
      int k = el < 0 ? 0 : el / (CP_STEP * T_STREAM / 512);
      k = k < NCP ? k : NCP;
      if (k >= knext) {
        const int d = (int)(ld4<SC1>(rws, lane < 8 ? lane * 4 : OOB, WS_CLK + k * 128) - (unsigned)tstart) - (k ? T_OFF + k * (CP_STEP * T_STREAM / 512) : 0);
        const bool ok = lane < 8 && (unsigned)(d + 600) < 2000u;
        const int n = __builtin_popcountll(__builtin_amdgcn_ballot_w64(ok));
        const int mean = __builtin_amdgcn_readfirstlane((int)(sum8(ok ? (float)d : 0.f) / (float)n));
        if (n >= 6) {
          int sl = k >= 2 && k == knext ? (mean - shift) * (1024 / CP_STEP) : 0;
          sl = sl < -150 ? -150 : (sl > 400 ? 400 : sl);
          knext = k + 1;
          shift = mean;
          if (lane == 0) *(volatile LAS v2u*)((LAS float*)lds + L_CNT + C_SCHED) = v2u{((unsigned)tstart + T_OFF + mean - ((k * CP_STEP * sl) >> 10)) | 1u, (unsigned)sl};
        }
      }
    };
    if (a == NA - 1) monitor();
    asm volatile("" :: "v"(pf));
    asm volatile("" : "+s"(ka));
    const u16 *b1 = (const u16*)ka[10], *b2 = (const u16*)ka[11];
    const int nu = (h >> 3) == 3 ? 3 : 2;
    const int f0 = (h & 7) * 576 + (h >> 3) * 128;
    __builtin_amdgcn_s_setprio(3);
    {
      const rsrc_t r1 = mk_rsrc(w1, DM * DF * 2);
      const int vo = s * (DF * 2) + e * 16;
#pragma unroll 1
      for (int u = 0; u < nu; ++u) {
        const int lo = 8 * a * (DF * 2) + f0 * 2 + u * 128;
        int off = lo + b * (64 * DF * 2);
        const unsigned eta = (unsigned)(now() - tstart) + (2 * nu + 2 - u) * (9 * T_WLOAD);
#pragma unroll
        for (int i = 0; i < 9; ++i) { one_or_two(eta); wb[i] = ld16<0>(r1, vo, off); CYC(off, 64 * DF * 2, lo, DM * DF * 2); }
        SCHED_FENCE();
        v4f d = {0.f, 0.f, 0.f, 0.f};
#pragma unroll
        for (int i = 0; i < 9; ++i) { d = mfma(af[i], wb[i], d); SCHED_FENCE(); }
        if (valid) *(v4f*)(lds + L_HP + (u * 16 + part) * 64 + dcol) = d;
        if (a == NA - 1) monitor();
      }
    }
    lds_arrive(cnt + C_HID, lane);
    const int vo2 = s * (DM * 2) + e * 16;
    {
      v4f am[9];
#pragma unroll
      for (int t = 0; t < 9; ++t) am[t] = v4f{0.f, 0.f, 0.f, 0.f};
      const rsrc_t r2 = mk_rsrc(w2 + (size_t)f0 * DM, nu * 64 * DM * 2);
      const rsrc_t rb1 = mk_rsrc(b1 + f0, nu * 128);
#pragma unroll 1
      for (int u = 0; u < nu; ++u) {
        const unsigned eta = (unsigned)(now() - tstart) + (nu + 2 - u) * (9 * T_WLOAD);
        const unsigned bias = __builtin_amdgcn_raw_buffer_load_b16(rb1, s * 2, u * 128 + a * 16, 0);
#pragma unroll
        for (int t = 0; t < 9; ++t) { if (t) one_or_two(eta); wb[t] = ld16<0>(r2, vo2, (u * 64 + 8 * a) * (DM * 2) + t * 128); }
        SCHED_FENCE();
        lds_wait<1>(cnt + C_HID, NA);
        const float* hp = lds + L_HP + (u * 16 + 2 * e) * 64 + 8 * a + s;
        float hv = sum8(hp[0] + hp[64]) + lo16(bias);
        const float uu = 0.7978845608028654f * (hv + 0.044715f * hv * hv * hv);
        hv = hv / (1.0f + __expf(-2.0f * uu));
        const unsigned rep = pack_bf16(hv, hv);
        const v4u ah = AFRAG(rep);
#pragma unroll
        for (int t = 0; t < 9; ++t) { am[t] = mfma(ah, wb[t], am[t]); SCHED_FENCE(); }
        if (a == NA - 1) monitor();
      }
      if (valid) {
#pragma unroll
        for (int t = 0; t < 9; ++t) *(v4f*)(lds + L_OUT + part * OSTR + dcol + t * 64) = am[t];
      }
    }
    lds_arrive(cnt + C_MLP, lane);
    const unsigned bb = ld4<0>(mk_rsrc(b2, h == 0 ? DM * 2 : 0), ht * 4, 0);
    {
      const rsrc_t r3 = mk_rsrc(wo + (size_t)h * DH * DM, DH * DM * 2);
      const unsigned eta = (unsigned)(now() - tstart) + 18 * T_WLOAD;
      const int vo3 = g * (DM * 2) + c * 16;
#pragma unroll
      for (int q = 0; q < 4; ++q)
#pragma unroll
        for (int t = 0; t < 4; ++t) {
          if (q && t == 0 && a == NA - 1) monitor();
          one_or_two(eta);
          wb[4 * q + t] = ld16<0>(r3, vo3, (16 * a + 4 * q) * (DM * 2) + t * 256);
        }
#pragma unroll
      for (int k = 0; k < 2; ++k) { one_or_two(eta); wb[16 + k] = ld16<0>(r3, vo2, (16 * a + 8 * k) * (DM * 2) + 1024); }
    }
    SCHED_FENCE();
    __builtin_amdgcn_s_setprio(0);
    const int tq = ht < 288 ? ht : 287;
    v2f mlp = v2f{lo16(bb), hi16(bb)};
    lds_wait<1>(cnt + C_MLP, NA);
#pragma unroll
    for (int p = 0; p < 16; ++p) mlp += *(const v2f*)(lds + L_OUT + p * OSTR + 2 * tq);
    mlp *= 1.0f - gate;
    PIN(mlp);
#pragma unroll
    for (int i = 0; i < 18; ++i) PIN(wb[i]);
    if (a == NA - 1) {
      __builtin_amdgcn_s_setprio(3);
      while (knext <= NCP && (int)(now() - tstart) < T_STREAM * 9 / 10) { monitor(); __builtin_amdgcn_s_sleep(30); }
    }
    float fac;
    {
      lds_wait<16>(cnt + C_ML, NS);
      const v4f m01 = *(const v4f*)(lds + L_ML), m23 = *(const v4f*)(lds + L_ML + 4);
      const float M = fmaxf(fmaxf(m01[0], m01[2]), fmaxf(m23[0], m23[2]));
      float L = __builtin_amdgcn_exp2f(m01[0] - M) * m01[1];
      L = fmaf(__builtin_amdgcn_exp2f(m01[2] - M), m01[3], L);
      L = fmaf(__builtin_amdgcn_exp2f(m23[0] - M), m23[1], L);
      L = fmaf(__builtin_amdgcn_exp2f(m23[2] - M), m23[3], L);
      fac = __builtin_amdgcn_exp2f(lds[L_ML + (e >> 1) * 2] - M) / L;
      PIN(fac);
    }
    int gvo = (ht >> 4) * (NH * 256) + (ht & 15) * 16;
    unsigned mlo[4], mhi[4];
#pragma unroll
    for (int r = 0; r < 4; ++r) { mlo[r] = c < 8 ? msk[r] : 0u; mhi[r] = c < 8 ? 0u : msk[r]; PIN(mlo[r]); PIN(mhi[r]); }
    LAS float* l3 = (LAS float*)lds;
    LAS float* pp = l3 + L_PART + e * PSTR + 16 * a + 8 * (c >> 3) + g;
    LAS v4f* ow = (LAS v4f*)(l3 + L_OUT + a * OSTR + 128 * (g >> 1) + 8 * c + 4 * (g & 1));
    LAS v4f* o8 = (LAS v4f*)(l3 + (valid ? L_OUT + part * OSTR + dcol + 512 : L_HP + lane * 4));
    LAS v2f* rd = (LAS v2f*)(l3 + L_OUT + (a == 4 ? 512 + 2 * (lane & 31) + (lane >> 5) * (8 * OSTR) : 2 * tq));
    asm volatile("" : "+v"(gvo), "+v"(pp), "+v"(ow), "+v"(o8), "+v"(rd), "+v"(tlo), "+v"(thi));
    __builtin_amdgcn_s_setprio(3);
    SCHED_FENCE();
    __syncthreads();
    {
      float t0 = pp[0] * fac, t1 = pp[4] * fac;
      PIN(t0); PIN(t1);
      t0 += dpp<XOR7>(t0); PIN(t0); t1 += dpp<XOR7>(t1); PIN(t1);
      t0 += dpp<XOR2>(t0); PIN(t0); t1 += dpp<XOR2>(t1); PIN(t1);
      t0 += dpp<XOR1>(t0); PIN(t0); t1 += dpp<XOR1>(t1); PIN(t1);
      const unsigned own = pack_bf16(t0, t1);
      const unsigned oth = dppu<XOR8>(own);
      const unsigned p01 = c < 8 ? own : oth, p23 = c < 8 ? oth : own;
      const unsigned rq[4] = {__builtin_amdgcn_perm(p01, p01, 0x01000100u), __builtin_amdgcn_perm(p01, p01, 0x03020302u),
                              __builtin_amdgcn_perm(p23, p23, 0x01000100u), __builtin_amdgcn_perm(p23, p23, 0x03020302u)};
      v4u fl[4], fh[4];
#pragma unroll
      for (int q = 0; q < 4; ++q) {
        fl[q] = v4u{rq[q] & mlo[0], rq[q] & mlo[1], rq[q] & mlo[2], rq[q] & mlo[3]};
        fh[q] = v4u{rq[q] & mhi[0], rq[q] & mhi[1], rq[q] & mhi[2], rq[q] & mhi[3]};
      }
      v4f d0 = {0.f, 0.f, 0.f, 0.f}, d1 = {0.f, 0.f, 0.f, 0.f};
#pragma unroll
      for (int q = 0; q < 4; ++q) { d0 = mfma(fl[q], wb[4 * q], d0); d0 = mfma(fh[q], wb[4 * q + 1], d0); }
      const v4f d8 = mfma(fl[2] | fh[3], wb[17], mfma(fl[0] | fh[1], wb[16], v4f{0.f, 0.f, 0.f, 0.f}));
      ow[0] = d0;
#pragma unroll
      for (int q = 0; q < 4; ++q) { d1 = mfma(fl[q], wb[4 * q + 2], d1); d1 = mfma(fh[q], wb[4 * q + 3], d1); }
      *o8 = d8;
      ow[64] = d1;
    }
    __syncthreads();
    if (a < 5) {
      constexpr int RS = OSTR / 2;
      v2f t = ((rd[0] + rd[RS]) + (rd[2 * RS] + rd[3 * RS])) + ((rd[4 * RS] + rd[5 * RS]) + (rd[6 * RS] + rd[7 * RS]));
      if (a == 4) t = v2f{sum_x32(t[0]), sum_x32(t[1])};
      const v4u gr = make_granules(fmaf(gate, t[0], mlp[0]), fmaf(gate, t[1], mlp[1]), tlo, thi);
      if (ht < 288) __builtin_amdgcn_raw_buffer_store_b128(gr, rws, gvo, WS_GO + b * (NCONS * NH * 256) + h * 256, SC1);
    }
  } else {
    int lane = tid & 63;
    asm volatile("; streaming" : "+v"(lane) :: "memory");
    unsigned char* ws = (unsigned char*)kv[1];
    __builtin_amdgcn_s_setprio(2);
    const int sw = wave;
    const int c = lane & 15;
    constexpr int HB = SK * DH * 2;
    const rsrc_t rk = mk_rsrc((const char*)kv[0] + (size_t)bid * HB, HB);
    const int vo = lane * 16 + sw * 1024;
    v4u buf[W], pre[W];
    SCHED_FENCE();
#pragma unroll
    for (int j = 0; j < 3; ++j) pre[j] = ld16<NTL>(rk, vo, page_j(j));
    SCHED_FENCE();
    unsigned d0 = 0, d1 = 0, d2 = 0, d3 = 0, d4 = 0, d5 = 0;
    if ((bid >> 3) == 2) {
      const u64 p01 = (sw & 1) ? kv[5] : kv[4], p23 = (sw & 1) ? kv[7] : kv[6], q01 = (sw & 1) ? kv[3] : kv[2];
      const u64 p = sw < 2 ? p01 : p23, q = sw < 2 ? q01 : kv[1] + 2048;
      const int st = sw < 2 ? (DM * NH * DH * 2 - 4) / 12 * 4 : (DM * DF * 2 - 4) / 12 * 4;
      asm volatile("s_load_dword %0, %5, 0x0\n\ts_load_dword %1, %5, %6\n\ts_load_dword %2, %5, %7\n\ts_load_dword %3, %5, %8\n\ts_load_dword %4, %9, 0x0"
                   : "=&s"(d0), "=&s"(d1), "=&s"(d2), "=&s"(d3), "=&s"(d4) : "s"(p), "s"(st), "s"(2 * st), "s"(3 * st), "s"(q));
    }
    SCHED_FENCE();
    RAW_BARRIER();
    SCHED_FENCE();
    u64 tb64;
    v2q kw;
    asm volatile("s_memrealtime %0\n\ts_load_dwordx4 %1, %2, 0x40" : "=&s"(tb64), "=&s"(kw) : "s"(ka));
    SCHED_FENCE();
#pragma unroll
    for (int j = 3; j < W; ++j) { pre[j] = ld16<NTL>(rk, vo, page_j(j)); SCHED_FENCE(); }
    asm volatile("s_waitcnt lgkmcnt(0)" : "+s"(tb64), "+s"(kw), "+s"(d0), "+s"(d1), "+s"(d2), "+s"(d3), "+s"(d4));
    const u64 vbase = kw[0] + (size_t)bid * HB;
    if (sw == 0) asm volatile("s_load_dword %0, %1, 0x0" : "=&s"(d5) : "s"(vbase));
    SCHED_FENCE();
#pragma unroll
    for (int j = 0; j < W; ++j) { buf[j] = ld16<NTL>(rk, vo, page_blk(1) | page_j(j)); SCHED_FENCE(); }
    asm volatile("s_waitcnt lgkmcnt(0)" : "+s"(d5));
    const rsrc_t rv = mk_rsrc((const void*)vbase, HB);
    u16* out = (u16*)kw[1];
    const unsigned tb = (unsigned)tb64 + T_OFF;
    unsigned t0 = tb;
    const int npub = __builtin_amdgcn_readfirstlane(sw == 0 && h < 8 && b >= 1 && b <= NCP ? b * CP_STEP : -1);
    const rsrc_t rclk = mk_rsrc(ws + WS_CLK + b * 128 + (h & 7) * 4, 4);
#define BLOCK_START(n0)                                                                                                        \
  const v2u sh = *(volatile LAS v2u*)((LAS float*)lds + L_CNT + C_SCHED);                                                      \
  const unsigned ta = __builtin_amdgcn_readfirstlane(sh[0]);                                                                   \
  t0 = (ta ? ta : tb) + ((((n0) + 8) * (int)__builtin_amdgcn_readfirstlane(sh[1])) >> 10);                                     \
  if ((n0) == npub) __builtin_amdgcn_raw_buffer_store_b32((unsigned)now(), rclk, lane * 4, 0, SC1)
    lds_wait<0>(cnt + C_Q, 1);
    const v4u qp = *(const v4u*)(lds + L_QP + 4 * c);
    constexpr int NBLK = SK / NS / 4 / W;
    float s[NBLK];
#pragma unroll
    for (int i = 0; i < NBLK; ++i) s[i] = 0.f;
#define TR_STEP(xs, HALF, CTRL)                                     \
  _Pragma("unroll") for (int u = 0; u < (HALF); ++u) {              \
    const float lo_ = xs[u], hi_ = xs[u + (HALF)];                  \
    const float t1_ = lo_ + dpp<CTRL>(lo_), t2_ = hi_ + dpp<CTRL>(hi_); \
    xs[u] = (c & (HALF)) ? t2_ : t1_;                               \
  }
#define TR(xs) TR_STEP(xs, 8, XOR8) TR_STEP(xs, 4, XOR7) TR_STEP(xs, 2, XOR2) TR_STEP(xs, 1, XOR1)
#define K_DOT(cur, dst)                                             \
  {                                                                 \
    float d = dot2(cur[0], qp[0], 0.f);                             \
    d = dot2(cur[1], qp[1], d);                                     \
    d = dot2(cur[2], qp[2], d);                                     \
    d = dot2(cur[3], qp[3], d);                                     \
    PIN(d);                                                         \
    dst = d;                                                        \
  }
    {
      float xs[W], x0[W];
      int lim = 0;
#pragma unroll
      for (int j = 0; j < W; ++j) {
        if ((j & 3) == 0) lim = pick_lim(t0, 2 * W + j, sw + (j >> 2));
        const v4u cur = buf[j];
        K_DOT(cur, xs[j])
        wait_lim(lim);
        buf[j] = ld16<NTL>(rk, vo, page_blk(2) | page_j(j));
        SCHED_FENCE();
        const v4u old = pre[j];
        K_DOT(old, x0[j])
        SCHED_FENCE();
      }
      TR(xs)
      TR(x0)
      s[0] = x0[0];
      s[1] = xs[0];
    }
#pragma unroll 1
    for (int blk = 2; blk < NBLK; ++blk) {
      const bool last = blk + 1 == NBLK;
      const rsrc_t rn = last ? rv : rk;
      const int so = page_blk(blk + 1);
      BLOCK_START((blk + 1) * W);
      float xs[W];
      int lim = 0;
#pragma unroll
      for (int j = 0; j < W; ++j) {
        if ((j & 3) == 0) lim = pick_lim(t0, (blk + 1) * W + j, sw + (j >> 2));
        const v4u cur = buf[j];
        K_DOT(cur, xs[j])
        wait_lim(lim);
        buf[j] = ld16<NTL>(rn, vo, so | page_j(j));
        SCHED_FENCE();
      }
      TR(xs)
#pragma unroll
      for (int i = 2; i + 1 < NBLK; ++i) s[i] = s[i + 1];
      s[NBLK - 1] = xs[0];
    }
#undef TR
#undef TR_STEP
#undef K_DOT
    constexpr float C2 = 0.088388347648318440f * 1.4426950408889634f;
    unsigned pp[NBLK];
    {
      float m = s[0];
#pragma unroll
      for (int i = 1; i < NBLK; ++i) m = fmaxf(m, s[i]);
      m = wave_max(m);
      float l = 0.f;
#pragma unroll
      for (int i = 0; i < NBLK; ++i) {
        const float p = __builtin_amdgcn_exp2f((s[i] - m) * C2);
        l += p;
        const float ph = hi16(__builtin_bit_cast(unsigned, p));
        pp[i] = pack_bf16(ph, p - ph);
      }
      l = wave_sum(l);
      if (lane == 0) *(v2f*)(lds + L_ML + 2 * sw) = v2f{m * C2, l};
      lds_arrive(cnt + C_ML, lane);
    }
    unsigned sel[4];
    {
      const unsigned src = (c & 8) ? 0x0302u : 0x0100u;
      const unsigned pl = (c & 1) ? (src << 16) | 0x0c0cu : 0x0c0c0000u | src;
#pragma unroll
      for (int r = 0; r < 4; ++r) sel[r] = ((c & 7) >> 1) == r ? pl : 0x0c0c0c0cu;
    }
    v4f acc = {0.f, 0.f, 0.f, 0.f};
#define V_STEP(J, REFILL)                                           \
  {                                                                 \
    const unsigned pb = dppu<BCAST + (J)>(p);                       \
    v4u af;                                                         \
    _Pragma("unroll") for (int r = 0; r < 4; ++r) af[r] = __builtin_amdgcn_perm(pb, pb, sel[r]); \
    acc = mfma(af, buf[J], acc);                                    \
    PIN(acc);                                                       \
    if (REFILL) {                                                   \
      if (((J) & 3) == 0) lim = pick_lim(t0, (NBLK + blk + 1) * W + (J), sw + ((J) >> 2)); \
      wait_lim(lim);                                                \
      buf[J] = ld16<NTL>(rv, vo, so | page_j(J));                   \
    }                                                               \
    SCHED_FENCE();                                                  \
  }
#define V_BLOCK(REFILL)                                             \
  V_STEP(0, REFILL) V_STEP(1, REFILL) V_STEP(2, REFILL) V_STEP(3, REFILL)     \
  V_STEP(4, REFILL) V_STEP(5, REFILL) V_STEP(6, REFILL) V_STEP(7, REFILL)     \
  V_STEP(8, REFILL) V_STEP(9, REFILL) V_STEP(10, REFILL) V_STEP(11, REFILL)   \
  V_STEP(12, REFILL) V_STEP(13, REFILL) V_STEP(14, REFILL) V_STEP(15, REFILL)
#pragma unroll 1
    for (int blk = 0; blk + 1 < NBLK; ++blk) {
      const int so = page_blk(blk + 1);
      BLOCK_START((NBLK + blk + 1) * W);
      const unsigned p = pp[0];
      int lim = 0;
      V_BLOCK(true)
#pragma unroll
      for (int i = 0; i + 1 < NBLK; ++i) pp[i] = pp[i + 1];
    }
    LAS v4f* pw = (LAS v4f*)((LAS float*)lds + L_PART + (2 * sw + (lane >> 5)) * PSTR + c * 8 + ((lane >> 4) & 1) * 4);
    PIN(pw);
    {
      const int so = 0, blk = 0;
      const unsigned p = pp[0];
      int lim = 0;
      V_BLOCK(false)
    }
#undef V_BLOCK
#undef BLOCK_START
#undef V_STEP
    *pw = acc;
    SCHED_FENCE();
    __syncthreads();
    SCHED_FENCE();
    const rsrc_t rws = mk_rsrc(ws, WS_END);
    const u64 z = *(const u64*)(lds + L_TAG);
    int go = (lane >> 2) * 256 + (lane & 3) * 16;
    GAS unsigned* op = (GAS unsigned*)(out + b * DM + 32 * h + 8 * sw) + (lane & 3);
    asm volatile("" : "+v"(go), "+v"(op));
    RAW_BARRIER();
    if (h < NCONS) {
      const unsigned tlo = (unsigned)z & 0xff000000u, thi = (unsigned)(z >> 32);
      const int so = WS_GO + (b * NCONS + h) * (NH * 256) + sw * 64;
      sleep_until(now() + T_POLL);
      v4u v0, v1;
      for (unsigned spins = 0;; ++spins) {
        v0 = ld16<SC1>(rws, go, so);
        v1 = ld16<SC1>(rws, go + 4096, so);
        if (__all(granules_ok(v0, tlo, thi) & granules_ok(v1, tlo, thi))) break;
        if (spins > SPIN_CAP) { v0[0] = v0[2] = 0x7fc000u; break; }
        asm volatile("" ::: "memory");
      }
      const unsigned a0 = v0[0], a1 = v0[2], b0 = v1[0], b1_ = v1[2];
      float r0 = granule_val(a0) + granule_val(b0), r1 = granule_val(a1) + granule_val(b1_);
      r0 += dpp<ROR4>(r0); r1 += dpp<ROR4>(r1);
      r0 += dpp<XOR8>(r0); r1 += dpp<XOR8>(r1);
      r0 = sum_x32(sum_x16(r0));
      r1 = sum_x32(sum_x16(r1));
      if (lane < 4) *op = pack_bf16(r0, r1);
      if (h == 0 && sw == 0 && lane == 0) __hip_atomic_store((GAS u64*)(ws + b * 128), z, __ATOMIC_RELAXED, __HIP_MEMORY_SCOPE_AGENT);
    }
  }
}

}

extern "C" void kernel_launch(void* const* d_in, const int* in_sizes, int n_in, void* d_out, int out_size,
                              void* d_ws, size_t ws_size, hipStream_t stream) {
  using namespace gm;
  const u16* x = (const u16*)d_in[0];
  const u16* kc = (const u16*)d_in[1];
  const u16* vc = (const u16*)d_in[2];
  const u16* wq = (const u16*)d_in[3];
  const u16* wo = (const u16*)d_in[4];
  const u16* w1 = (const u16*)d_in[6];
  const u16* b1 = (const u16*)d_in[7];
  const u16* w2 = (const u16*)d_in[8];
  const u16* b2 = (const u16*)d_in[9];
  const u16* gw = (const u16*)d_in[10];
  hipLaunchKernelGGL(fused_main, dim3(NB * NH), dim3(NT), 0, stream, kc, (unsigned char*)d_ws, x, gw, wq, wo, w1, w2, vc, (u16*)d_out, b1, b2);
}
```
